```python
import torch

def _softmax(x, axis=-1):
    return torch.softmax(x, dim=axis)
def _gelu(x):
    return torch.nn.functional.gelu(x, approximate="tanh")
def _silu(x):
    return torch.nn.functional.silu(x)
def _sigmoid(x):
    return torch.sigmoid(x)
def _layernorm(x, axis=-1, eps=1e-5):
    m = x.mean(dim=axis, keepdim=True)
    v = x.var(dim=axis, keepdim=True, unbiased=False)
    return (x - m) / torch.sqrt(v + eps)
def _scatter_add(tgt, idx, src, axis=0):
    return tgt.clone().index_add_(axis, idx, src)
def _segment_sum(src, seg, n):
    out = torch.zeros((n,) + src.shape[1:], dtype=src.dtype)
    return out.index_add_(0, seg, src)
def _pad(x, pw):
    flat = tuple(v for pair in reversed(pw) for v in pair)
    return torch.nn.functional.pad(x, flat)

kernel_name = 'standalone__self_sup_conv_stem_hubert__speech'

def setup_inputs():

    _w = (lambda a: (lambda t: t.to(torch.bfloat16) if t.is_floating_point() else t)(torch.as_tensor(a)))
    B, T, D, K = 2, 4000, 512, 5
    scale = (1.0 / (K * D)) ** 0.5
    return {
        "x": _w((lambda *s: torch.randn(*s, dtype=torch.bfloat16) * 0.05)(B, T, D)),
        "w": _w((lambda *s: torch.randn(*s, dtype=torch.bfloat16) * 0.05)(K, D, D) * scale),
        "b": _w((lambda *s: torch.randn(*s, dtype=torch.bfloat16) * 0.05)(D) * 0.1),
        "gamma": _w(torch.ones((D,), dtype=float)),
        "beta": _w(torch.zeros((D,), dtype=float)),
    }

def reference(x, w, b, gamma, beta):
    B, T, D = x.shape
    K = w.shape[0]
    s = 2
    L = (T - K) // s + 1
    y = torch.einsum('blc,cd->bld', x[:, 0 : s * L : s, :], w[0])
    for i in range(1, K):
        y = y + torch.einsum('blc,cd->bld', x[:, i : i + s * L : s, :], w[i])
    y = y + b[None, None, :]
    mu = y.mean(1, keepdims=True)
    var = ((y - mu) ** 2).mean(1, keepdims=True)
    yn = (y - mu) / torch.sqrt(var + 1e-5)
    yn = yn * gamma[None, None, :] + beta[None, None, :]
    return _gelu(yn)
```

```cpp
#include <hip/hip_runtime.h>
#include <hip/hip_bf16.h>
#include <stdint.h>

typedef __attribute__((ext_vector_type(8))) short bf16x8;
typedef __attribute__((ext_vector_type(4))) float f32x4;

static __device__ __forceinline__ float bf2f(unsigned short v) { return __uint_as_float(((unsigned)v) << 16); }

constexpr int B_ = 2, T_ = 4000, D_ = 512, K_ = 5, L_ = 1998;

__global__ void gemm_simple(const unsigned short* x, const unsigned short* w, const unsigned short* bias, float* y) {
    int lane = threadIdx.x & 63;
    int wave = threadIdx.x >> 6;
    int ct = blockIdx.y * 4 + wave;
    int rt = blockIdx.x;
    int b = rt / 125, l0 = (rt % 125) * 16;
    int r = lane & 15, q = lane >> 4;
    int l = l0 + r; if (l > L_ - 1) l = L_ - 1;
    f32x4 acc = {0, 0, 0, 0};
    const unsigned short* xa = x + ((size_t)b * T_ + 2 * l) * D_;
    for (int k0 = 0; k0 < K_ * D_; k0 += 32) {
        bf16x8 a = *(const bf16x8*)(xa + k0 + q * 8);
        bf16x8 bb;
        for (int j = 0; j < 8; ++j) bb[j] = (short)w[(size_t)(k0 + q * 8 + j) * D_ + ct * 16 + r];
        acc = __builtin_amdgcn_mfma_f32_16x16x32_bf16(a, bb, acc, 0, 0, 0);
    }
    for (int j = 0; j < 4; ++j) {
        int ll = l0 + q * 4 + j;
        int d = ct * 16 + r;
        if (ll < L_) y[((size_t)b * L_ + ll) * D_ + d] = acc[j] + bf2f(bias[d]);
    }
}

__global__ void stats_k(const float* y, float* mu, float* rs) {
    int b = blockIdx.y; int d = blockIdx.x * 64 + (threadIdx.x & 63); int g = threadIdx.x >> 6;
    __shared__ float s[16][64];
    float a = 0;
    for (int l = g; l < L_; l += 16) a += y[((size_t)b * L_ + l) * D_ + d];
    s[g][threadIdx.x & 63] = a; __syncthreads();
    float m = 0; for (int i = 0; i < 16; ++i) m += s[i][threadIdx.x & 63];
    m /= L_; __syncthreads();
    a = 0;
    for (int l = g; l < L_; l += 16) { float t = y[((size_t)b * L_ + l) * D_ + d] - m; a += t * t; }
    s[g][threadIdx.x & 63] = a; __syncthreads();
    float v = 0; for (int i = 0; i < 16; ++i) v += s[i][threadIdx.x & 63];
    v /= L_;
    if (g == 0) { mu[b * D_ + d] = m; rs[b * D_ + d] = 1.0f / sqrtf(v + 1e-5f); }
}

__global__ void norm_k(const float* y, const float* mu, const float* rs, const unsigned short* gamma, const unsigned short* beta, __hip_bfloat16* out) {
    int i = blockIdx.x * 256 + threadIdx.x;
    if (i >= B_ * L_ * D_) return;
    int d = i % D_; int b = i / (L_ * D_);
    float v = (y[i] - mu[b * D_ + d]) * rs[b * D_ + d] * bf2f(gamma[d]) + bf2f(beta[d]);
    float u = 0.7978845608028654f * (v + 0.044715f * v * v * v);
    float t = tanhf(u);
    out[i] = __float2bfloat16(0.5f * v * (1.0f + t));
}

extern "C" void kernel_launch(void* const* d_in, const int* in_sizes, int n_in,
                              void* d_out, int out_size, void* d_ws, size_t ws_size,
                              hipStream_t stream) {
    const unsigned short* x = (const unsigned short*)d_in[0];
    const unsigned short* w = (const unsigned short*)d_in[1];
    const unsigned short* bias = (const unsigned short*)d_in[2];
    const unsigned short* gamma = (const unsigned short*)d_in[3];
    const unsigned short* beta = (const unsigned short*)d_in[4];
    float* y = (float*)d_ws;
    float* mu = y + (size_t)B_ * L_ * D_;
    float* rs = mu + B_ * D_;
    gemm_simple<<<dim3(250, 8), 256, 0, stream>>>(x, w, bias, y);
    stats_k<<<dim3(8, 2), 1024, 0, stream>>>(y, mu, rs);
    norm_k<<<(B_ * L_ * D_ + 255) / 256, 256, 0, stream>>>(y, mu, rs, gamma, beta, (__hip_bfloat16*)d_out);
}
```

```cpp
#include <hip/hip_runtime.h>
#include <stdint.h>

typedef unsigned short u16;
typedef unsigned long long u64;
typedef __attribute__((ext_vector_type(8))) __bf16 bf16x8;
typedef __attribute__((ext_vector_type(2))) __bf16 bf16x2;
typedef __attribute__((ext_vector_type(4))) short s16x4;
typedef __attribute__((ext_vector_type(8))) short s16x8;
typedef __attribute__((ext_vector_type(16))) float f32x16;
typedef __attribute__((ext_vector_type(4))) float f32x4;
typedef __attribute__((ext_vector_type(2))) float f32x2;
typedef __attribute__((ext_vector_type(4))) unsigned u32x4;
#define LAS __attribute__((address_space(3)))
#define DI __device__ __forceinline__

constexpr int T_ = 4000, D_ = 512, L_ = 1998, TS = 125;

constexpr unsigned XS = 0;
constexpr unsigned WE = 65536;
constexpr unsigned WO = WE + 49152;
constexpr unsigned MISC = WO + 32768;
constexpr unsigned L_EP = MISC;
constexpr unsigned L_ST = MISC + 64;
constexpr unsigned L_AC = L_ST + 4 * 512;
constexpr unsigned LDS_BYTES = L_AC + 8 * 512;
constexpr unsigned EX = 0;
constexpr unsigned YB = 32768;
constexpr unsigned YPL = 16384 + 64;

constexpr size_t WS_EPOCH = 0;
constexpr size_t WS_FLAG = 4096;
constexpr size_t WS_BOX = 65536;
#define STAMP(i) do {} while (0)

#define ROT_EXPR ((R + 2 * ct) & 7)
#define PER_A 2
#define PER_B 2
#define SPLIT 7
#define PRO 1
#define STPOL "sc1"
constexpr u64 BOX_EMPTY = 0xFFFFFFFFFFFFFFFFull;
constexpr unsigned SPIN_MAX = 1u << 16;

DI float bf2f(u16 v) { return __uint_as_float(((unsigned)v) << 16); }
DI u64 ld_dev(const u64* p) { return __hip_atomic_load(p, __ATOMIC_RELAXED, __HIP_MEMORY_SCOPE_AGENT); }
DI void st_dev(u64* p, u64 v) { __hip_atomic_store(p, v, __ATOMIC_RELAXED, __HIP_MEMORY_SCOPE_AGENT); }
DI float half_sum(float v) {
    const unsigned u = __float_as_uint(v);
    const auto r = __builtin_amdgcn_permlane32_swap(u, u, false, false);
    return __uint_as_float(r[0]) + __uint_as_float(r[1]);
}
#define BAR() asm volatile("s_barrier" ::: "memory")
#define LGKM_BAR() asm volatile("s_waitcnt lgkmcnt(0)\n\ts_barrier" ::: "memory")
#define VM_BARN(n) asm volatile("s_waitcnt vmcnt(%0)\n\ts_barrier" :: "n"(n) : "memory")
#define VM_BAR(n) asm volatile("s_waitcnt vmcnt(" #n ")\n\ts_barrier" ::: "memory")

#define XPOL ""
#define WPOL ""
DI void dmax(unsigned ldsaddr, unsigned voff, __amdgpu_buffer_rsrc_t srd, unsigned soff) {
    asm volatile("s_mov_b32 m0, %0\n\ts_nop 0\n\tbuffer_load_dwordx4 %1, %2, %3 offen " XPOL " lds"
                 :: "s"(ldsaddr), "v"(voff), "s"(srd), "s"(soff) : "m0", "memory");
}
DI void dmaw(unsigned ldsaddr, unsigned voff, __amdgpu_buffer_rsrc_t srd, unsigned soff) {
    asm volatile("s_mov_b32 m0, %0\n\ts_nop 0\n\tbuffer_load_dwordx4 %1, %2, %3 offen " WPOL " lds"
                 :: "s"(ldsaddr), "v"(voff), "s"(srd), "s"(soff) : "m0", "memory");
}

struct Frag { bf16x8 a[6]; bf16x8 b[3]; };

DI bf16x8 ld_a(const LAS unsigned char* p) { return *(const LAS bf16x8*)p; }
DI bf16x8 ld_b(const LAS unsigned char* p) {
    const s16x4 lo = __builtin_amdgcn_ds_read_tr16_b64_v4i16((LAS s16x4*)p);
    const s16x4 hi = __builtin_amdgcn_ds_read_tr16_b64_v4i16((LAS s16x4*)(p + 512));
    const s16x8 r = __builtin_shufflevector(lo, hi, 0, 1, 2, 3, 4, 5, 6, 7);
    return __builtin_bit_cast(bf16x8, r);
}

template <int S, int STEP>
DI void load_frag(Frag& f, const LAS unsigned char* const (&pa)[2][2], const LAS unsigned char* pwe, const LAS unsigned char* pwo) {
    constexpr bool EVEN = (S & 1) == 0;
    constexpr int NA = EVEN ? 6 : 5, NB = EVEN ? 3 : 2;
    const LAS unsigned char* pw = EVEN ? pwe + (S >> 1) * 24576 + STEP * 2048 : pwo + (S >> 1) * 16384 + STEP * 2048;
    f.b[0] = ld_b(pw);
#pragma unroll
    for (int i = 0; i < 4; ++i) f.a[i] = ld_a(pa[0][STEP] + S * 16384 + i * 4096);
    f.b[1] = ld_b(pw + 8192);
#pragma unroll
    for (int i = 4; i < NA; ++i) f.a[i] = ld_a(pa[1][STEP] + S * 16384 + (i - 4) * 4096);
    if constexpr (NB == 3) f.b[2] = ld_b(pw + 16384);
}
template <int S>
DI void mma_frag(const Frag& f, f32x16 (&acc)[4]) {
    constexpr int NB = (S & 1) == 0 ? 3 : 2;
#pragma unroll
    for (int s = 0; s < NB; ++s)
#pragma unroll
        for (int j = 0; j < 4; ++j) acc[j] = __builtin_amdgcn_mfma_f32_32x32x16_bf16(f.a[j + s], f.b[s], acc[j], 0, 0, 0);
}
template <int NM, int ND, int PER>
DI void interleave() {
    constexpr int FULL = ND / PER < NM ? ND / PER : NM;
    constexpr int REST = ND - FULL * PER;
#pragma unroll
    for (int i = 0; i < FULL; ++i) {
        __builtin_amdgcn_sched_group_barrier(0x008, 1, 0);
        __builtin_amdgcn_sched_group_barrier(0x100, PER, 0);
    }
    if constexpr (REST > 0 && FULL < NM) {
        __builtin_amdgcn_sched_group_barrier(0x008, 1, 0);
        __builtin_amdgcn_sched_group_barrier(0x100, REST, 0);
        if constexpr (NM - FULL - 1 > 0) __builtin_amdgcn_sched_group_barrier(0x008, NM - FULL - 1, 0);
    } else {
        if constexpr (REST > 0) __builtin_amdgcn_sched_group_barrier(0x100, REST, 0);
        if constexpr (NM > FULL) __builtin_amdgcn_sched_group_barrier(0x008, NM - FULL, 0);
    }
}

__global__ void __launch_bounds__(512, 2)
conv_stem_fused(const u16* __restrict__ x, const u16* __restrict__ w, const u16* __restrict__ bias,
                const u16* __restrict__ gamma, const u16* __restrict__ beta, u16* __restrict__ out,
                unsigned char* __restrict__ ws) {
    __shared__ __attribute__((aligned(16))) unsigned char smem[LDS_BYTES];
    LAS unsigned char* L = (LAS unsigned char*)smem;
    const unsigned lbase = (unsigned)(size_t)L;

    const int tid = threadIdx.x, lane = tid & 63;
    const int wave = __builtin_amdgcn_readfirstlane(tid >> 6);
    const int bid = blockIdx.x, xcd = bid & 7, idx = bid >> 3;
    const int R = 8 * (xcd >> 1) + (idx & 7), ct = 4 * (xcd & 1) + (idx >> 3);
    const int b = R >> 4, rt = R & 15, grp = b * 8 + ct;
    const int l0 = rt * TS, nv = rt == 15 ? L_ - 15 * TS : TS;
    u64* const box = (u64*)(ws + WS_BOX) + (size_t)grp * 2048;
    u64* const flags = (u64*)(ws + WS_FLAG) + grp * 16;
    u64* const epoch = (u64*)(ws + WS_EPOCH + grp * 256);
    STAMP(0);

    const float gam = bf2f(gamma[ct * 64 + lane]), bet = bf2f(beta[ct * 64 + lane]);

    if (wave >= 4) {
        const int lw = wave - 4;
        const __amdgpu_buffer_rsrc_t sx = __builtin_amdgcn_make_buffer_rsrc((void*)x, 0, 2 * T_ * D_ * 2, 0x00020000);
        const __amdgpu_buffer_rsrc_t sw = __builtin_amdgcn_make_buffer_rsrc((void*)w, 0, 5 * D_ * D_ * 2, 0x00020000);
        const int r8 = lane >> 3, c8 = lane & 7, ent = 8 * lw + r8;
        unsigned vx[4];
#pragma unroll
        for (int n = 0; n < 4; ++n) {
            const int p = min(l0 + 4 * ent + n, T_ / 2 - 1);
            vx[n] = p * 2048 + ((c8 ^ ((ent >> 1) & 7)) << 4);
        }
        const unsigned vw = r8 * 1024 + ((c8 ^ (((r8 >> 1) & 1) << 2)) << 4);
        const unsigned sx0 = b * (T_ * D_ * 2), sw0 = ct * 128 + lw * 8192;
        const unsigned mx0 = lbase + XS + lw * 1024, mwe0 = lbase + WE + lw * 1024, mwo0 = lbase + WO + lw * 1024;
        const int rot = ROT_EXPR;
        auto issue_e = [&](int cc, int lo, int hi) {
            const int cr = (cc + rot) & 7;
            const unsigned so = sx0 + cr * 128, mx = mx0 + (cc & 1) * 32768, mw = mwe0 + (cc & 1) * 24576, wo = sw0 + cr * 65536;
#pragma unroll
            for (int n = 0; n < 4; ++n) if (n >= lo && n < hi) dmax(mx + n * 4096, vx[n], sx, so);
#pragma unroll
            for (int ti = 0; ti < 3; ++ti)
#pragma unroll
                for (int h = 0; h < 2; ++h) if (4 + 2 * ti + h >= lo && 4 + 2 * ti + h < hi) dmaw(mw + ti * 8192 + h * 4096, vw, sw, wo + ti * 2 * 524288 + h * 32768);
        };
        auto issue_o = [&](int cc) {
            const int cr = (cc + rot) & 7;
            const unsigned so = sx0 + cr * 128 + 1024, mx = mx0 + (cc & 1) * 32768 + 16384, mw = mwo0 + (cc & 1) * 16384, wo = sw0 + cr * 65536 + 524288;
#pragma unroll
            for (int n = 0; n < 4; ++n) dmax(mx + n * 4096, vx[n], sx, so);
#pragma unroll
            for (int ti = 0; ti < 2; ++ti)
#pragma unroll
                for (int h = 0; h < 2; ++h) dmaw(mw + ti * 8192 + h * 4096, vw, sw, wo + ti * 2 * 524288 + h * 32768);
        };
        issue_e(0, 0, 10); issue_o(0);
        STAMP(1);
        VM_BAR(8);
        STAMP(2);
        issue_e(1, 0, 10); VM_BAR(10);
        issue_o(1); VM_BAR(8);
        issue_e(2, 0, 10); issue_o(2); VM_BAR(18);
        issue_e(3, 0, SPLIT); VM_BARN(8 + SPLIT);
        for (int cc = 2; cc < 6; ++cc) {
            issue_e(cc + 1, SPLIT, 10); issue_o(cc + 1); VM_BAR(18);
            issue_e(cc + 2, 0, SPLIT); VM_BARN(8 + SPLIT);
        }
        issue_e(7, SPLIT, 10); issue_o(7); VM_BAR(18);
        VM_BAR(8);
        VM_BAR(0);
        STAMP(3);
        const u64 e1 = *(volatile LAS u64*)(L + L_EP) + 1;
        u64 fl = lane < 16 ? ld_dev(flags + lane) : e1;
        BAR();
        STAMP(4);
        for (unsigned sp = 0; __any(fl != e1) && sp < SPIN_MAX; ++sp) fl = lane < 16 ? ld_dev(flags + lane) : e1;
        if (rt == 0 && lw == 0 && lane == 0) st_dev(epoch, e1);
        const u64* src = box + lw * 512 + lane;
        u64 g[8];
        for (unsigned sp = 0; sp < SPIN_MAX; ++sp) {
            bool bad = false;
#pragma unroll
            for (int i = 0; i < 8; ++i) g[i] = ld_dev(src + i * 64);
#pragma unroll
            for (int i = 0; i < 8; ++i) bad |= (unsigned)(g[i] >> 32) == 0xFFFFFFFFu;
            if (!__any(bad)) break;
        }
        STAMP(5);
        float n[8], s = 0.f, nt = 0.f;
#pragma unroll
        for (int i = 0; i < 8; ++i) {
            n[i] = (float)((i & 1 ? 62 : 63) - ((lw == 3 && i >= 6) ? 1 : 0));
            s += n[i] * __uint_as_float((unsigned)g[i]); nt += n[i];
        }
        const float mp = s / nt;
        float q = 0.f;
#pragma unroll
        for (int i = 0; i < 8; ++i) { const float d = __uint_as_float((unsigned)g[i]) - mp; q += __uint_as_float((unsigned)(g[i] >> 32)) + n[i] * d * d; }
        *(LAS f32x2*)(L + L_ST + lw * 512 + lane * 8) = f32x2{mp, q};
    } else {
        const int kh = wave & 1, chh = wave >> 1;
        const int n = lane & 31, hi = lane >> 5;
        u64 e0 = 0;
        if (wave == 0) {
            e0 = ld_dev(epoch);
            st_dev(box + rt * 128 + lane, BOX_EMPTY);
            st_dev(box + rt * 128 + 64 + lane, BOX_EMPTY);
        }
        f32x16 acc[4];
        const float b0 = kh == 0 ? bf2f(bias[ct * 64 + chh * 32 + n]) : 0.f;
#pragma unroll
        for (int j = 0; j < 4; ++j)
#pragma unroll
            for (int i = 0; i < 16; ++i) acc[j][i] = b0;
        const LAS unsigned char* pa[2][2];
#pragma unroll
        for (int u = 0; u < 2; ++u)
#pragma unroll
            for (int st = 0; st < 2; ++st) {
                const int pos = n + u, ks = 2 * kh + st;
                pa[u][st] = L + XS + pos * 128 + (((2 * ks + hi) ^ ((pos >> 1) & 7)) << 4);
            }
        const int g4 = lane >> 4, q4 = (lane & 15) >> 2, p4 = lane & 3;
        const unsigned wl = kh * 4096 + (8 * (g4 >> 1) + q4) * 128 + (((4 * chh + 2 * (g4 & 1) + (p4 >> 1)) ^ ((q4 >> 1) << 2)) << 4) + 8 * (p4 & 1);
        const LAS unsigned char* pwe = L + WE + wl;
        const LAS unsigned char* pwo = L + WO + wl;
        Frag f0, f1;
        BAR();
        STAMP(1);
        load_frag<0, 0>(f0, pa, pwe, pwo);
        for (int it = 0; it < 4; ++it) {
#define STAGE(S, LAST)                                                                                         \
            load_frag<S, 1>(f1, pa, pwe, pwo); mma_frag<S>(f0, acc);                                          \
            interleave<(S & 1) ? 8 : 12, (S & 1) ? 9 : 12, PER_A>();                                                  \
            if (!(LAST)) { __builtin_amdgcn_sched_barrier(0); LGKM_BAR(); __builtin_amdgcn_sched_barrier(0); load_frag<(S + 1) & 3, 0>(f0, pa, pwe, pwo); }                          \
            mma_frag<S>(f1, acc);                                                                              \
            if (!(LAST)) interleave<(S & 1) ? 8 : 12, (S & 1) ? 12 : 9, PER_B>();
            STAGE(0, false) STAGE(1, false) STAGE(2, false)
            if (it < 3) { STAGE(3, false) } else { STAGE(3, true) }
#undef STAGE
            if (it == 0 && wave == 0) {
                asm volatile("s_waitcnt vmcnt(0)" ::: "memory");
                if (lane == 0) { *(volatile LAS u64*)(L + L_EP) = e0; st_dev(flags + rt, e0 + 1); }
            }
        }
        STAMP(2);
        f32x16 y[2];
        {
            LAS unsigned char* snd = L + EX + wave * 8192 + lane * 16;
            const LAS unsigned char* rcv = L + EX + (wave ^ 1) * 8192 + lane * 16;
            if (kh == 0) {
#pragma unroll
                for (int jj = 0; jj < 2; ++jj)
#pragma unroll
                    for (int q = 0; q < 4; ++q) *(LAS f32x4*)(snd + (jj * 4 + q) * 1024) = f32x4{acc[2 + jj][4 * q], acc[2 + jj][4 * q + 1], acc[2 + jj][4 * q + 2], acc[2 + jj][4 * q + 3]};
                y[0] = acc[0]; y[1] = acc[1];
            } else {
#pragma unroll
                for (int jj = 0; jj < 2; ++jj)
#pragma unroll
                    for (int q = 0; q < 4; ++q) *(LAS f32x4*)(snd + (jj * 4 + q) * 1024) = f32x4{acc[jj][4 * q], acc[jj][4 * q + 1], acc[jj][4 * q + 2], acc[jj][4 * q + 3]};
                y[0] = acc[2]; y[1] = acc[3];
            }
            LGKM_BAR();
#pragma unroll
            for (int jj = 0; jj < 2; ++jj)
#pragma unroll
                for (int q = 0; q < 4; ++q) {
                    const f32x4 v = *(const LAS f32x4*)(rcv + (jj * 4 + q) * 1024);
#pragma unroll
                    for (int e = 0; e < 4; ++e) y[jj][4 * q + e] += v[e];
                }
        }
        STAMP(3);
        {
            bool ok[2][2];
#pragma unroll
            for (int jj = 0; jj < 2; ++jj) { ok[jj][0] = hi == 0 || 120 + 2 * kh + jj < nv; ok[jj][1] = hi == 0 || 124 + 2 * kh + jj < nv; }
            float s0 = 0.f, s1 = 0.f;
#pragma unroll
            for (int i = 0; i < 14; ++i) { s0 += y[0][i]; s1 += y[1][i]; }
#pragma unroll
            for (int i = 14; i < 16; ++i) { s0 += ok[0][i - 14] ? y[0][i] : 0.f; s1 += ok[1][i - 14] ? y[1][i] : 0.f; }
            const float cnt = (float)((kh ? 62 : 63) - (rt == 15 ? 1 : 0));
            const float m = half_sum(s0 + s1) / cnt;
            float q0 = 0.f, q1 = 0.f;
#pragma unroll
            for (int i = 0; i < 14; ++i) { const float d0 = y[0][i] - m, d1 = y[1][i] - m; q0 += d0 * d0; q1 += d1 * d1; }
#pragma unroll
            for (int i = 14; i < 16; ++i) {
                const float d0 = ok[0][i - 14] ? y[0][i] - m : 0.f, d1 = ok[1][i - 14] ? y[1][i] - m : 0.f;
                q0 += d0 * d0; q1 += d1 * d1;
            }
            const float m2 = half_sum(q0 + q1);
            unsigned m2u = __float_as_uint(m2);
            if (m2u == 0xFFFFFFFFu) m2u = 0x7FFFFFFFu;
            if (hi == 0) st_dev(box + rt * 128 + kh * 64 + chh * 32 + n, ((u64)m2u << 32) | __float_as_uint(m));
        }
        STAMP(4);
        {
            const int c = chh * 32 + n;
            LAS unsigned char* yp = L + YB + ((c >> 2) & 1) * YPL + (16 * hi + 2 * kh) * 128 + ((c >> 3) * 4 + (c & 3)) * 4;
#pragma unroll
            for (int jj = 0; jj < 2; ++jj)
#pragma unroll
                for (int i = 0; i < 16; ++i) *(LAS float*)(yp + (32 * (i >> 2) + 4 * (i & 3) + jj) * 128) = y[jj][i];
        }
    }
    LGKM_BAR();
    STAMP(6);
    const int r8 = lane >> 3, cg = lane & 7;
    f32x4 v[2][2];
#pragma unroll
    for (int u = 0; u < 2; ++u)
#pragma unroll
        for (int p = 0; p < 2; ++p) v[u][p] = *(const LAS f32x4*)(L + YB + p * YPL + (16 * wave + 8 * u + r8) * 128 + cg * 16);
    {
        f32x2 st[4];
#pragma unroll
        for (int i = 0; i < 4; ++i) st[i] = *(const LAS f32x2*)(L + L_ST + i * 512 + lane * 8);
        constexpr float n0 = 500.f, n3 = 498.f;
        const float mean = (n0 * (st[0][0] + st[1][0] + st[2][0]) + n3 * st[3][0]) * (1.f / L_);
        const float d0 = st[0][0] - mean, d1 = st[1][0] - mean, d2 = st[2][0] - mean, d3 = st[3][0] - mean;
        const float m2 = (st[0][1] + st[1][1]) + (st[2][1] + st[3][1]) + n0 * (d0 * d0 + d1 * d1 + d2 * d2) + n3 * d3 * d3;
        const float a = gam / sqrtf(m2 * (1.f / L_) + 1e-5f);
        LAS float* ac = (LAS float*)(L + L_AC + wave * 512);
        ac[lane] = a; ac[64 + lane] = bet - mean * a;
    }
    asm volatile("s_waitcnt lgkmcnt(0)" ::: "memory");
    f32x4 sa[2], sc[2];
#pragma unroll
    for (int p = 0; p < 2; ++p) {
        sa[p] = *(const LAS f32x4*)(L + L_AC + wave * 512 + cg * 32 + p * 16);
        sc[p] = *(const LAS f32x4*)(L + L_AC + wave * 512 + 256 + cg * 32 + p * 16);
    }
    constexpr float K1 = -2.0f * 1.4426950408889634f * 0.7978845608028654f, K2 = K1 * 0.044715f;
#pragma unroll
    for (int u = 0; u < 2; ++u) {
        const int t = 16 * wave + 8 * u + r8;
        unsigned o[4];
#pragma unroll
        for (int p = 0; p < 2; ++p) {
            float r[4];
#pragma unroll
            for (int e = 0; e < 4; ++e) {
                const float z = v[u][p][e] * sa[p][e] + sc[p][e];
                const float ex = __builtin_amdgcn_exp2f(z * (K1 + K2 * z * z));
                r[e] = z * __builtin_amdgcn_rcpf(1.f + ex);
            }
            o[2 * p] = __builtin_bit_cast(unsigned, bf16x2{(__bf16)r[0], (__bf16)r[1]});
            o[2 * p + 1] = __builtin_bit_cast(unsigned, bf16x2{(__bf16)r[2], (__bf16)r[3]});
        }
        if (t < nv) {
            const u32x4 ov = u32x4{o[0], o[1], o[2], o[3]};
            u16* op = out + ((size_t)(b * L_ + l0 + t) * D_ + ct * 64 + cg * 8);
            asm volatile("global_store_dwordx4 %0, %1, off " STPOL :: "v"(op), "v"(ov) : "memory");
        }
    }
    STAMP(7);
}

extern "C" void kernel_launch(void* const* d_in, const int* in_sizes, int n_in, void* d_out, int out_size,
                              void* d_ws, size_t ws_size, hipStream_t stream) {
    conv_stem_fused<<<256, 512, 0, stream>>>((const u16*)d_in[0], (const u16*)d_in[1], (const u16*)d_in[2],
                                             (const u16*)d_in[3], (const u16*)d_in[4], (u16*)d_out, (unsigned char*)d_ws);
}
```

```cpp
#include <hip/hip_runtime.h>
#include <stdint.h>

typedef unsigned short u16;
typedef unsigned long long u64;
typedef __attribute__((ext_vector_type(8))) __bf16 bf16x8;
typedef __attribute__((ext_vector_type(2))) __bf16 bf16x2;
typedef __attribute__((ext_vector_type(4))) short s16x4;
typedef __attribute__((ext_vector_type(8))) short s16x8;
typedef __attribute__((ext_vector_type(16))) float f32x16;
typedef __attribute__((ext_vector_type(4))) float f32x4;
typedef __attribute__((ext_vector_type(2))) float f32x2;
typedef __attribute__((ext_vector_type(4))) unsigned u32x4;
#define LAS __attribute__((address_space(3)))
#define DI __device__ __forceinline__

constexpr int T_ = 4000, D_ = 512, L_ = 1998, TS = 125;

constexpr unsigned XS = 0;
constexpr unsigned WE = 65536;
constexpr unsigned WO = WE + 49152;
constexpr unsigned MISC = WO + 32768;
constexpr unsigned L_EP = MISC;
constexpr unsigned L_ST = MISC + 64;
constexpr unsigned L_AC = L_ST + 4 * 512;
constexpr unsigned L_TP = L_AC + 8 * 512;
constexpr unsigned LDS_BYTES = L_TP + 64;
constexpr unsigned EX = 0;
constexpr unsigned YB = 32768;
constexpr unsigned YPL = 16384 + 64;

constexpr size_t WS_EPOCH = 0;
constexpr size_t WS_FLAG = 4096;
constexpr size_t WS_HINT = 8192;
constexpr size_t WS_BOX = 65536;
#define STAMP(i) do {} while (0)

#define ROT_EXPR ((R + 2 * ct) & 7)
#define PER_A 2
#define PER_B 2
#define SPLIT 7
#define MARGIN 15
#define PRO 1
#define STPOL "sc1"
constexpr u64 BOX_EMPTY = 0xFFFFFFFFFFFFFFFFull;
constexpr unsigned SPIN_MAX = 1u << 16;

DI float bf2f(u16 v) { return __uint_as_float(((unsigned)v) << 16); }
DI u64 ld_dev(const u64* p) { return __hip_atomic_load(p, __ATOMIC_RELAXED, __HIP_MEMORY_SCOPE_AGENT); }
DI void st_dev(u64* p, u64 v) { __hip_atomic_store(p, v, __ATOMIC_RELAXED, __HIP_MEMORY_SCOPE_AGENT); }
DI float half_sum(float v) {
    const unsigned u = __float_as_uint(v);
    const auto r = __builtin_amdgcn_permlane32_swap(u, u, false, false);
    return __uint_as_float(r[0]) + __uint_as_float(r[1]);
}
#define BAR() asm volatile("s_barrier" ::: "memory")
#define LGKM_BAR() asm volatile("s_waitcnt lgkmcnt(0)\n\ts_barrier" ::: "memory")
#define VM_BARN(n) asm volatile("s_waitcnt vmcnt(%0)\n\ts_barrier" :: "n"(n) : "memory")
#define VM_BAR(n) asm volatile("s_waitcnt vmcnt(" #n ")\n\ts_barrier" ::: "memory")

#define XPOL ""
#define WPOL ""
DI void dmax(unsigned ldsaddr, unsigned voff, __amdgpu_buffer_rsrc_t srd, unsigned soff) {
    asm volatile("s_mov_b32 m0, %0\n\ts_nop 0\n\tbuffer_load_dwordx4 %1, %2, %3 offen " XPOL " lds"
                 :: "s"(ldsaddr), "v"(voff), "s"(srd), "s"(soff) : "m0", "memory");
}
DI void dmaw(unsigned ldsaddr, unsigned voff, __amdgpu_buffer_rsrc_t srd, unsigned soff) {
    asm volatile("s_mov_b32 m0, %0\n\ts_nop 0\n\tbuffer_load_dwordx4 %1, %2, %3 offen " WPOL " lds"
                 :: "s"(ldsaddr), "v"(voff), "s"(srd), "s"(soff) : "m0", "memory");
}

struct Frag { bf16x8 a[6]; bf16x8 b[3]; };

DI bf16x8 ld_a(const LAS unsigned char* p) { return *(const LAS bf16x8*)p; }
DI bf16x8 ld_b(const LAS unsigned char* p) {
    const s16x4 lo = __builtin_amdgcn_ds_read_tr16_b64_v4i16((LAS s16x4*)p);
    const s16x4 hi = __builtin_amdgcn_ds_read_tr16_b64_v4i16((LAS s16x4*)(p + 512));
    const s16x8 r = __builtin_shufflevector(lo, hi, 0, 1, 2, 3, 4, 5, 6, 7);
    return __builtin_bit_cast(bf16x8, r);
}

template <int S, int STEP>
DI void load_frag(Frag& f, const LAS unsigned char* const (&pa)[2][2], const LAS unsigned char* pwe, const LAS unsigned char* pwo) {
    constexpr bool EVEN = (S & 1) == 0;
    constexpr int NA = EVEN ? 6 : 5, NB = EVEN ? 3 : 2;
    const LAS unsigned char* pw = EVEN ? pwe + (S >> 1) * 24576 + STEP * 2048 : pwo + (S >> 1) * 16384 + STEP * 2048;
    f.b[0] = ld_b(pw);
#pragma unroll
    for (int i = 0; i < 4; ++i) f.a[i] = ld_a(pa[0][STEP] + S * 16384 + i * 4096);
    f.b[1] = ld_b(pw + 8192);
#pragma unroll
    for (int i = 4; i < NA; ++i) f.a[i] = ld_a(pa[1][STEP] + S * 16384 + (i - 4) * 4096);
    if constexpr (NB == 3) f.b[2] = ld_b(pw + 16384);
}
template <int S>
DI void mma_frag(const Frag& f, f32x16 (&acc)[4]) {
    constexpr int NB = (S & 1) == 0 ? 3 : 2;
#pragma unroll
    for (int s = 0; s < NB; ++s)
#pragma unroll
        for (int j = 0; j < 4; ++j) acc[j] = __builtin_amdgcn_mfma_f32_32x32x16_bf16(f.a[j + s], f.b[s], acc[j], 0, 0, 0);
}
template <int NM, int ND, int PER>
DI void interleave() {
    constexpr int FULL = ND / PER < NM ? ND / PER : NM;
    constexpr int REST = ND - FULL * PER;
#pragma unroll
    for (int i = 0; i < FULL; ++i) {
        __builtin_amdgcn_sched_group_barrier(0x008, 1, 0);
        __builtin_amdgcn_sched_group_barrier(0x100, PER, 0);
    }
    if constexpr (REST > 0 && FULL < NM) {
        __builtin_amdgcn_sched_group_barrier(0x008, 1, 0);
        __builtin_amdgcn_sched_group_barrier(0x100, REST, 0);
        if constexpr (NM - FULL - 1 > 0) __builtin_amdgcn_sched_group_barrier(0x008, NM - FULL - 1, 0);
    } else {
        if constexpr (REST > 0) __builtin_amdgcn_sched_group_barrier(0x100, REST, 0);
        if constexpr (NM > FULL) __builtin_amdgcn_sched_group_barrier(0x008, NM - FULL, 0);
    }
}

__global__ void __launch_bounds__(512, 2)
conv_stem_fused(const u16* __restrict__ x, const u16* __restrict__ w, const u16* __restrict__ bias,
                const u16* __restrict__ gamma, const u16* __restrict__ beta, u16* __restrict__ out,
                unsigned char* __restrict__ ws) {
    __shared__ __attribute__((aligned(16))) unsigned char smem[LDS_BYTES];
    LAS unsigned char* L = (LAS unsigned char*)smem;
    const unsigned lbase = (unsigned)(size_t)L;

    const int tid = threadIdx.x, lane = tid & 63;
    const int wave = __builtin_amdgcn_readfirstlane(tid >> 6);
    const int bid = blockIdx.x, xcd = bid & 7, idx = bid >> 3;
    const int R = 8 * (xcd >> 1) + (idx & 7), ct = 4 * (xcd & 1) + (idx >> 3);
    const int b = R >> 4, rt = R & 15, grp = b * 8 + ct;
    const int l0 = rt * TS, nv = rt == 15 ? L_ - 15 * TS : TS;
    u64* const box = (u64*)(ws + WS_BOX) + (size_t)grp * 2048;
    u64* const flags = (u64*)(ws + WS_FLAG) + grp * 16;
    u64* const epoch = (u64*)(ws + WS_EPOCH + grp * 256);
    u64* const hint = (u64*)(ws + WS_HINT) + grp * 256;
    STAMP(0);

    const float gam = bf2f(gamma[ct * 64 + lane]), bet = bf2f(beta[ct * 64 + lane]);

    if (wave >= 4) {
        const int lw = wave - 4;
        const __amdgpu_buffer_rsrc_t sx = __builtin_amdgcn_make_buffer_rsrc((void*)x, 0, 2 * T_ * D_ * 2, 0x00020000);
        const __amdgpu_buffer_rsrc_t sw = __builtin_amdgcn_make_buffer_rsrc((void*)w, 0, 5 * D_ * D_ * 2, 0x00020000);
        const int r8 = lane >> 3, c8 = lane & 7, ent = 8 * lw + r8;
        unsigned vx[4];
#pragma unroll
        for (int n = 0; n < 4; ++n) {
            const int p = min(l0 + 4 * ent + n, T_ / 2 - 1);
            vx[n] = p * 2048 + ((c8 ^ ((ent >> 1) & 7)) << 4);
        }
        const unsigned vw = r8 * 1024 + ((c8 ^ (((r8 >> 1) & 1) << 2)) << 4);
        const unsigned sx0 = b * (T_ * D_ * 2), sw0 = ct * 128 + lw * 8192;
        const unsigned mx0 = lbase + XS + lw * 1024, mwe0 = lbase + WE + lw * 1024, mwo0 = lbase + WO + lw * 1024;
        const int rot = ROT_EXPR;
        auto issue_e = [&](int cc, int lo, int hi) {
            const int cr = (cc + rot) & 7;
            const unsigned so = sx0 + cr * 128, mx = mx0 + (cc & 1) * 32768, mw = mwe0 + (cc & 1) * 24576, wo = sw0 + cr * 65536;
#pragma unroll
            for (int n = 0; n < 4; ++n) if (n >= lo && n < hi) dmax(mx + n * 4096, vx[n], sx, so);
#pragma unroll
            for (int ti = 0; ti < 3; ++ti)
#pragma unroll
                for (int h = 0; h < 2; ++h) if (4 + 2 * ti + h >= lo && 4 + 2 * ti + h < hi) dmaw(mw + ti * 8192 + h * 4096, vw, sw, wo + ti * 2 * 524288 + h * 32768);
        };
        auto issue_o = [&](int cc) {
            const int cr = (cc + rot) & 7;
            const unsigned so = sx0 + cr * 128 + 1024, mx = mx0 + (cc & 1) * 32768 + 16384, mw = mwo0 + (cc & 1) * 16384, wo = sw0 + cr * 65536 + 524288;
#pragma unroll
            for (int n = 0; n < 4; ++n) dmax(mx + n * 4096, vx[n], sx, so);
#pragma unroll
            for (int ti = 0; ti < 2; ++ti)
#pragma unroll
                for (int h = 0; h < 2; ++h) dmaw(mw + ti * 8192 + h * 4096, vw, sw, wo + ti * 2 * 524288 + h * 32768);
        };
        issue_e(0, 0, 10); issue_o(0);
        STAMP(1);
        VM_BAR(8);
        STAMP(2);
        issue_e(1, 0, 10); VM_BAR(10);
        issue_o(1); VM_BAR(8);
        issue_e(2, 0, 10); issue_o(2); VM_BAR(18);
        issue_e(3, 0, SPLIT); VM_BARN(8 + SPLIT);
        for (int cc = 2; cc < 6; ++cc) {
            issue_e(cc + 1, SPLIT, 10); issue_o(cc + 1); VM_BAR(18);
            issue_e(cc + 2, 0, SPLIT); VM_BARN(8 + SPLIT);
        }
        issue_e(7, SPLIT, 10); issue_o(7); VM_BAR(18);
        VM_BAR(8);
        VM_BAR(0);
        STAMP(3);
        const u64 e1 = *(volatile LAS u64*)(L + L_EP) + 1;
        u64 fl = lane < 16 ? ld_dev(flags + lane) : e1;
        const unsigned th = (unsigned)__builtin_amdgcn_s_memrealtime();
        if (lw == 0 && lane == 0) st_dev(hint + rt * 16, (e1 << 32) | th);
        BAR();
        STAMP(4);
        for (unsigned sp = 0; __any(fl != e1) && sp < SPIN_MAX; ++sp) fl = lane < 16 ? ld_dev(flags + lane) : e1;
        if (rt == 0 && lw == 0 && lane == 0) st_dev(epoch, e1);
        volatile LAS unsigned* tpw = (volatile LAS unsigned*)(L + L_TP);
        if (lw == 0) {
            u64 h = (e1 << 32) | th;
            for (unsigned sp = 0; sp < SPIN_MAX; ++sp) {
                if (lane < 16) h = ld_dev(hint + lane * 16);
                if (!__any((unsigned)(h >> 32) != (unsigned)e1)) break;
            }
            int d = (unsigned)(h >> 32) == (unsigned)e1 ? (int)((unsigned)h - th) : 0;
#pragma unroll
            for (int o = 1; o < 16; o <<= 1) d = max(d, __shfl_xor(d, o));
            d = min(max(d, 0), 1000);
            if (lane == 0) tpw[4] = 0x80000000u | (unsigned)d;
        }
        unsigned tp = 0, dl = 0;
        for (unsigned sp = 0; sp < SPIN_MAX * 16; ++sp) {
            tp = tpw[lw]; dl = tpw[4];
            if (tp != 0 && dl != 0) break;
            __builtin_amdgcn_s_sleep(1);
        }
        const unsigned tstart = tp + (dl & 0xFFFFu) + MARGIN;
        while ((int)((unsigned)__builtin_amdgcn_s_memrealtime() - tstart) < 0) {}
        const u64* src = box + lw * 512 + lane;
        u64 g[8];
#pragma unroll
        for (int i = 0; i < 8; ++i) g[i] = BOX_EMPTY;
        STAMP(8);
        for (unsigned sp = 0; sp < SPIN_MAX; ++sp) {
            bool bad = false;
#pragma unroll
            for (int i = 0; i < 8; ++i) g[i] = ld_dev(src + i * 64);
#pragma unroll
            for (int i = 0; i < 8; ++i) bad |= (unsigned)(g[i] >> 32) == 0xFFFFFFFFu;
            const bool more = __any(bad);
            if (!more) break;
        }
        STAMP(5);
        float n[8], s = 0.f, nt = 0.f;
#pragma unroll
        for (int i = 0; i < 8; ++i) {
            n[i] = (float)((i & 1 ? 62 : 63) - ((lw == 3 && i >= 6) ? 1 : 0));
            s += n[i] * __uint_as_float((unsigned)g[i]); nt += n[i];
        }
        const float mp = s / nt;
        float q = 0.f;
#pragma unroll
        for (int i = 0; i < 8; ++i) { const float d = __uint_as_float((unsigned)g[i]) - mp; q += __uint_as_float((unsigned)(g[i] >> 32)) + n[i] * d * d; }
        *(LAS f32x2*)(L + L_ST + lw * 512 + lane * 8) = f32x2{mp, q};
    } else {
        const int kh = wave & 1, chh = wave >> 1;
        const int n = lane & 31, hi = lane >> 5;
        u64 e0 = 0;
        if (wave == 0) {
            e0 = ld_dev(epoch);
            st_dev(box + rt * 128 + lane, BOX_EMPTY);
            st_dev(box + rt * 128 + 64 + lane, BOX_EMPTY);
            if (lane < 5) *(volatile LAS unsigned*)(L + L_TP + lane * 4) = 0;
        }
        f32x16 acc[4];
        const float b0 = kh == 0 ? bf2f(bias[ct * 64 + chh * 32 + n]) : 0.f;
#pragma unroll
        for (int j = 0; j < 4; ++j)
#pragma unroll
            for (int i = 0; i < 16; ++i) acc[j][i] = b0;
        const LAS unsigned char* pa[2][2];
#pragma unroll
        for (int u = 0; u < 2; ++u)
#pragma unroll
            for (int st = 0; st < 2; ++st) {
                const int pos = n + u, ks = 2 * kh + st;
                pa[u][st] = L + XS + pos * 128 + (((2 * ks + hi) ^ ((pos >> 1) & 7)) << 4);
            }
        const int g4 = lane >> 4, q4 = (lane & 15) >> 2, p4 = lane & 3;
        const unsigned wl = kh * 4096 + (8 * (g4 >> 1) + q4) * 128 + (((4 * chh + 2 * (g4 & 1) + (p4 >> 1)) ^ ((q4 >> 1) << 2)) << 4) + 8 * (p4 & 1);
        const LAS unsigned char* pwe = L + WE + wl;
        const LAS unsigned char* pwo = L + WO + wl;
        Frag f0, f1;
        BAR();
        STAMP(1);
        load_frag<0, 0>(f0, pa, pwe, pwo);
        for (int it = 0; it < 4; ++it) {
#define STAGE(S, LAST)                                                                                         \
            load_frag<S, 1>(f1, pa, pwe, pwo); mma_frag<S>(f0, acc);                                          \
            interleave<(S & 1) ? 8 : 12, (S & 1) ? 9 : 12, PER_A>();                                                  \
            if (!(LAST)) { __builtin_amdgcn_sched_barrier(0); LGKM_BAR(); __builtin_amdgcn_sched_barrier(0); load_frag<(S + 1) & 3, 0>(f0, pa, pwe, pwo); }                          \
            mma_frag<S>(f1, acc);                                                                              \
            if (!(LAST)) interleave<(S & 1) ? 8 : 12, (S & 1) ? 12 : 9, PER_B>();
            STAGE(0, false) STAGE(1, false) STAGE(2, false)
            if (it < 3) { STAGE(3, false) } else { STAGE(3, true) }
#undef STAGE
            if (it == 0 && wave == 0) {
                asm volatile("s_waitcnt vmcnt(0)" ::: "memory");
                if (lane == 0) { *(volatile LAS u64*)(L + L_EP) = e0; st_dev(flags + rt, e0 + 1); }
            }
        }
        STAMP(2);
        f32x16 y[2];
        {
            LAS unsigned char* snd = L + EX + wave * 8192 + lane * 16;
            const LAS unsigned char* rcv = L + EX + (wave ^ 1) * 8192 + lane * 16;
            if (kh == 0) {
#pragma unroll
                for (int jj = 0; jj < 2; ++jj)
#pragma unroll
                    for (int q = 0; q < 4; ++q) *(LAS f32x4*)(snd + (jj * 4 + q) * 1024) = f32x4{acc[2 + jj][4 * q], acc[2 + jj][4 * q + 1], acc[2 + jj][4 * q + 2], acc[2 + jj][4 * q + 3]};
                y[0] = acc[0]; y[1] = acc[1];
            } else {
#pragma unroll
                for (int jj = 0; jj < 2; ++jj)
#pragma unroll
                    for (int q = 0; q < 4; ++q) *(LAS f32x4*)(snd + (jj * 4 + q) * 1024) = f32x4{acc[jj][4 * q], acc[jj][4 * q + 1], acc[jj][4 * q + 2], acc[jj][4 * q + 3]};
                y[0] = acc[2]; y[1] = acc[3];
            }
            LGKM_BAR();
#pragma unroll
            for (int jj = 0; jj < 2; ++jj)
#pragma unroll
                for (int q = 0; q < 4; ++q) {
                    const f32x4 v = *(const LAS f32x4*)(rcv + (jj * 4 + q) * 1024);
#pragma unroll
                    for (int e = 0; e < 4; ++e) y[jj][4 * q + e] += v[e];
                }
        }
        STAMP(3);
        {
            bool ok[2][2];
#pragma unroll
            for (int jj = 0; jj < 2; ++jj) { ok[jj][0] = hi == 0 || 120 + 2 * kh + jj < nv; ok[jj][1] = hi == 0 || 124 + 2 * kh + jj < nv; }
            float s0 = 0.f, s1 = 0.f;
#pragma unroll
            for (int i = 0; i < 14; ++i) { s0 += y[0][i]; s1 += y[1][i]; }
#pragma unroll
            for (int i = 14; i < 16; ++i) { s0 += ok[0][i - 14] ? y[0][i] : 0.f; s1 += ok[1][i - 14] ? y[1][i] : 0.f; }
            const float cnt = (float)((kh ? 62 : 63) - (rt == 15 ? 1 : 0));
            const float m = half_sum(s0 + s1) / cnt;
            float q0 = 0.f, q1 = 0.f;
#pragma unroll
            for (int i = 0; i < 14; ++i) { const float d0 = y[0][i] - m, d1 = y[1][i] - m; q0 += d0 * d0; q1 += d1 * d1; }
#pragma unroll
            for (int i = 14; i < 16; ++i) {
                const float d0 = ok[0][i - 14] ? y[0][i] - m : 0.f, d1 = ok[1][i - 14] ? y[1][i] - m : 0.f;
                q0 += d0 * d0; q1 += d1 * d1;
            }
            const float m2 = half_sum(q0 + q1);
            unsigned m2u = __float_as_uint(m2);
            if (m2u == 0xFFFFFFFFu) m2u = 0x7FFFFFFFu;
            if (hi == 0) st_dev(box + rt * 128 + kh * 64 + chh * 32 + n, ((u64)m2u << 32) | __float_as_uint(m));
            const unsigned tp = (unsigned)__builtin_amdgcn_s_memrealtime() | 1u;
            if (lane == 0) *(volatile LAS unsigned*)(L + L_TP + wave * 4) = tp;
        }
        STAMP(4);
        {
            const int c = chh * 32 + n;
            LAS unsigned char* yp = L + YB + ((c >> 2) & 1) * YPL + (16 * hi + 2 * kh) * 128 + ((c >> 3) * 4 + (c & 3)) * 4;
#pragma unroll
            for (int jj = 0; jj < 2; ++jj)
#pragma unroll
                for (int i = 0; i < 16; ++i) *(LAS float*)(yp + (32 * (i >> 2) + 4 * (i & 3) + jj) * 128) = y[jj][i];
        }
    }
    LGKM_BAR();
    STAMP(6);
    const int r8 = lane >> 3, cg = lane & 7;
    f32x4 v[2][2];
#pragma unroll
    for (int u = 0; u < 2; ++u)
#pragma unroll
        for (int p = 0; p < 2; ++p) v[u][p] = *(const LAS f32x4*)(L + YB + p * YPL + (16 * wave + 8 * u + r8) * 128 + cg * 16);
    {
        f32x2 st[4];
#pragma unroll
        for (int i = 0; i < 4; ++i) st[i] = *(const LAS f32x2*)(L + L_ST + i * 512 + lane * 8);
        constexpr float n0 = 500.f, n3 = 498.f;
        const float mean = (n0 * (st[0][0] + st[1][0] + st[2][0]) + n3 * st[3][0]) * (1.f / L_);
        const float d0 = st[0][0] - mean, d1 = st[1][0] - mean, d2 = st[2][0] - mean, d3 = st[3][0] - mean;
        const float m2 = (st[0][1] + st[1][1]) + (st[2][1] + st[3][1]) + n0 * (d0 * d0 + d1 * d1 + d2 * d2) + n3 * d3 * d3;
        const float a = gam / sqrtf(m2 * (1.f / L_) + 1e-5f);
        LAS float* ac = (LAS float*)(L + L_AC + wave * 512);
        ac[lane] = a; ac[64 + lane] = bet - mean * a;
    }
    asm volatile("s_waitcnt lgkmcnt(0)" ::: "memory");
    f32x4 sa[2], sc[2];
#pragma unroll
    for (int p = 0; p < 2; ++p) {
        sa[p] = *(const LAS f32x4*)(L + L_AC + wave * 512 + cg * 32 + p * 16);
        sc[p] = *(const LAS f32x4*)(L + L_AC + wave * 512 + 256 + cg * 32 + p * 16);
    }
    constexpr float K1 = -2.0f * 1.4426950408889634f * 0.7978845608028654f, K2 = K1 * 0.044715f;
#pragma unroll
    for (int u = 0; u < 2; ++u) {
        const int t = 16 * wave + 8 * u + r8;
        unsigned o[4];
#pragma unroll
        for (int p = 0; p < 2; ++p) {
            float r[4];
#pragma unroll
            for (int e = 0; e < 4; ++e) {
                const float z = v[u][p][e] * sa[p][e] + sc[p][e];
                const float ex = __builtin_amdgcn_exp2f(z * (K1 + K2 * z * z));
                r[e] = z * __builtin_amdgcn_rcpf(1.f + ex);
            }
            o[2 * p] = __builtin_bit_cast(unsigned, bf16x2{(__bf16)r[0], (__bf16)r[1]});
            o[2 * p + 1] = __builtin_bit_cast(unsigned, bf16x2{(__bf16)r[2], (__bf16)r[3]});
        }
        if (t < nv) {
            const u32x4 ov = u32x4{o[0], o[1], o[2], o[3]};
            u16* op = out + ((size_t)(b * L_ + l0 + t) * D_ + ct * 64 + cg * 8);
            asm volatile("global_store_dwordx4 %0, %1, off " STPOL :: "v"(op), "v"(ov) : "memory");
        }
    }
    STAMP(7);
}

extern "C" void kernel_launch(void* const* d_in, const int* in_sizes, int n_in, void* d_out, int out_size,
                              void* d_ws, size_t ws_size, hipStream_t stream) {
    conv_stem_fused<<<256, 512, 0, stream>>>((const u16*)d_in[0], (const u16*)d_in[1], (const u16*)d_in[2],
                                             (const u16*)d_in[3], (const u16*)d_in[4], (u16*)d_out, (unsigned char*)d_ws);
}
```

```cpp
#include <hip/hip_runtime.h>
#include <stdint.h>

typedef unsigned short u16;
typedef unsigned long long u64;
typedef __attribute__((ext_vector_type(8))) __bf16 bf16x8;
typedef __attribute__((ext_vector_type(2))) __bf16 bf16x2;
typedef __attribute__((ext_vector_type(4))) short s16x4;
typedef __attribute__((ext_vector_type(8))) short s16x8;
typedef __attribute__((ext_vector_type(16))) float f32x16;
typedef __attribute__((ext_vector_type(4))) float f32x4;
typedef __attribute__((ext_vector_type(2))) float f32x2;
typedef __attribute__((ext_vector_type(4))) unsigned u32x4;
#define LAS __attribute__((address_space(3)))
#define DI __device__ __forceinline__

constexpr int T_ = 4000, D_ = 512, L_ = 1998, TS = 125;

constexpr unsigned XS = 0;
constexpr unsigned WE = 65536;
constexpr unsigned WO = WE + 49152;
constexpr unsigned MISC = WO + 32768;
constexpr unsigned L_EP = MISC;
constexpr unsigned L_AC = MISC + 64;
constexpr unsigned L_TP = L_AC + 512;
constexpr unsigned LDS_BYTES = L_TP + 64;
constexpr unsigned EX = 0;
constexpr unsigned YB = 32768;
constexpr unsigned YPL = 16384 + 64;

constexpr size_t WS_EPOCH = 0;
constexpr size_t WS_FLAG = 4096;
constexpr size_t WS_HINT = 8192;
constexpr size_t WS_BOX = 65536;
#define STAMP(i) do {} while (0)

#define ROT_EXPR ((R + 2 * ct) & 7)
#define PER_A 2
#define PER_B 2
#define SPLIT 7
#define MARGIN 15
#define PRO 1
#define STPOL "sc1"
constexpr u64 BOX_EMPTY = 0xFFFFFFFFFFFFFFFFull;
constexpr unsigned SPIN_MAX = 1u << 16;

DI float bf2f(u16 v) { return __uint_as_float(((unsigned)v) << 16); }
DI u64 ld_dev(const u64* p) { return __hip_atomic_load(p, __ATOMIC_RELAXED, __HIP_MEMORY_SCOPE_AGENT); }
DI void st_dev(u64* p, u64 v) { __hip_atomic_store(p, v, __ATOMIC_RELAXED, __HIP_MEMORY_SCOPE_AGENT); }
DI float half_sum(float v) {
    const unsigned u = __float_as_uint(v);
    const auto r = __builtin_amdgcn_permlane32_swap(u, u, false, false);
    return __uint_as_float(r[0]) + __uint_as_float(r[1]);
}
#define BAR() asm volatile("s_barrier" ::: "memory")
#define LGKM_BAR() asm volatile("s_waitcnt lgkmcnt(0)\n\ts_barrier" ::: "memory")
#define VM_BARN(n) asm volatile("s_waitcnt vmcnt(%0)\n\ts_barrier" :: "n"(n) : "memory")
#define VM_BAR(n) asm volatile("s_waitcnt vmcnt(" #n ")\n\ts_barrier" ::: "memory")

#define XPOL ""
#define WPOL ""
DI void dmax(unsigned ldsaddr, unsigned voff, __amdgpu_buffer_rsrc_t srd, unsigned soff) {
    asm volatile("s_mov_b32 m0, %0\n\ts_nop 0\n\tbuffer_load_dwordx4 %1, %2, %3 offen " XPOL " lds"
                 :: "s"(ldsaddr), "v"(voff), "s"(srd), "s"(soff) : "m0", "memory");
}
DI void dmaw(unsigned ldsaddr, unsigned voff, __amdgpu_buffer_rsrc_t srd, unsigned soff) {
    asm volatile("s_mov_b32 m0, %0\n\ts_nop 0\n\tbuffer_load_dwordx4 %1, %2, %3 offen " WPOL " lds"
                 :: "s"(ldsaddr), "v"(voff), "s"(srd), "s"(soff) : "m0", "memory");
}

DI float quad_sum(float v) {
    const unsigned u = __float_as_uint(v);
    const auto r = __builtin_amdgcn_permlane16_swap(u, u, false, false);
    return half_sum(__uint_as_float(r[0]) + __uint_as_float(r[1]));
}
DI bf16x8 ld_a(const LAS unsigned char* p) { return *(const LAS bf16x8*)p; }
DI bf16x8 ld_b(const LAS unsigned char* p) {
    const s16x4 lo = __builtin_amdgcn_ds_read_tr16_b64_v4i16((LAS s16x4*)p);
    const s16x4 hi = __builtin_amdgcn_ds_read_tr16_b64_v4i16((LAS s16x4*)(p + 512));
    const s16x8 r = __builtin_shufflevector(lo, hi, 0, 1, 2, 3, 4, 5, 6, 7);
    return __builtin_bit_cast(bf16x8, r);
}

struct Op { int a, s, u; };
constexpr Op op_of(int nt, int m) {
    int k = 0;
    for (int a = 0; a < 7 + nt; ++a)
        for (int s = 0; s < nt; ++s)
            if (a - s >= 0 && a - s < 8)
                for (int u = 1; u >= 0; --u) { if (k == m) return Op{a, s, u}; ++k; }
    return Op{-1, -1, -1};
}
#define BARL 27
constexpr int BAR_E = 36, BAR_O = 18, BAR_L = BARL;
constexpr int rd_even(int m) {
    switch (m) {
        case 2: return 4; case 7: return 5; case 12: return 6; case 17: return 7; case 21: return 8; case 25: return 9;
        case 36: return 21; case 37: return 20; case 38: return 10; case 39: return 11; case 40: return 23; case 41: return 22;
        case 42: return 12; case 43: return 13; case 45: return 14;
        default: return -1;
    }
}
constexpr int rd_odd(int m) {
    switch (m) {
        case 0: return 5; case 2: return 6; case 4: return 7; case 6: return 8;
        case 18: return 21; case 19: return 20; case 20: return 10; case 21: return 11; case 22: return 23; case 23: return 22;
        case 24: return 12; case 25: return 25; case 26: return 24; case 28: return 13;
        default: return -1;
    }
}
struct Ptr { unsigned xa, xb, we[2], wo[2]; };
DI const LAS unsigned char* lds_at(unsigned a) { return (const LAS unsigned char*)(size_t)a; }
template <bool ODD> DI bf16x8 rd_x(const Ptr& p, int a) {
    return a < 8 ? ld_a(lds_at(p.xa) + (ODD ? 16384 : 0) + a * 2048) : ld_a(lds_at(p.xb) + (ODD ? 16384 : 0) + (a - 8) * 2048);
}
template <bool ODD> DI bf16x8 rd_w(const Ptr& p, int s, int u) { return ld_b(lds_at(ODD ? p.wo[u] : p.we[u]) + s * 8192); }
DI void mma(f32x4& c, const bf16x8& a, const bf16x8& b) {
    asm volatile("v_mfma_f32_16x16x32_bf16 %0, %1, %2, %0" : "+v"(c) : "v"(a), "v"(b));
}
DI void flip(unsigned& a, int d) { asm volatile("v_add_u32 %0, %1, %0" : "+v"(a) : "s"(d)); }
DI void negate(int& d) { asm volatile("s_sub_i32 %0, 0, %0" : "+s"(d) :: "scc"); }
template <bool ODD, int M>
DI void stage_ops(f32x4 (&acc)[8][2], bf16x8 (&xa)[10], const bf16x8 (&wc)[3][2], bf16x8 (&wn)[3][2], Ptr& p, int& dx, int& de, int& dq) {
    constexpr int NT = ODD ? 2 : 3, NM = 16 * NT;
    if constexpr (M < NM) {
        if constexpr (M == (ODD ? BAR_O : BAR_E)) { LGKM_BAR(); __builtin_amdgcn_sched_barrier(0); }
        constexpr Op o = op_of(NT, M);
        mma(acc[o.a - o.s][o.u], xa[o.a], wc[o.s][o.u]);
        __builtin_amdgcn_sched_barrier(0);
        constexpr int rd = ODD ? rd_odd(M) : rd_even(M);
        if constexpr (rd >= 0 && rd < 10) xa[rd] = rd_x<ODD>(p, rd);
        if constexpr (rd >= 10 && rd < 20) xa[rd - 10] = rd_x<!ODD>(p, rd - 10);
        if constexpr (rd >= 20) wn[(rd - 20) >> 1][rd & 1] = rd_w<!ODD>(p, (rd - 20) >> 1, rd & 1);
        if constexpr (ODD && M == 8) flip(p.xa, dx);
        if constexpr (ODD && M == 9) flip(p.xb, dx);
        if constexpr (ODD && M == 10) flip(p.we[0], de);
        if constexpr (ODD && M == 11) flip(p.we[1], de);
        if constexpr (ODD && M == 12) flip(p.wo[0], dq);
        if constexpr (ODD && M == 13) flip(p.wo[1], dq);
        if constexpr (ODD && M == 14) { negate(dx); negate(de); }
        if constexpr (ODD && M == 15) negate(dq);
        __builtin_amdgcn_sched_barrier(0);
        stage_ops<ODD, M + 1>(acc, xa, wc, wn, p, dx, de, dq);
    }
}
template <int M>
DI void stage_last(f32x4 (&acc)[8][2], bf16x8 (&xa)[10], const bf16x8 (&wc)[3][2], f32x4 (&rc)[8], const Ptr& p,
                   LAS unsigned char* snd, const LAS unsigned char* rcv) {
    if constexpr (M < 32) {
        if constexpr (M == BAR_L) { LGKM_BAR(); __builtin_amdgcn_sched_barrier(0); }
        constexpr Op o = op_of(2, 2 * (M & 15));
        constexpr int u = M < 16 ? 1 : 0;
        mma(acc[o.a - o.s][u], xa[o.a], wc[o.s][u]);
        __builtin_amdgcn_sched_barrier(0);
        if constexpr (M < 4) xa[5 + M] = rd_x<true>(p, 5 + M);
        if constexpr (M >= 10 && M <= 22 && (M & 1) == 0) *(LAS f32x4*)(snd + ((M - 10) >> 1) * 1024) = acc[(M - 10) >> 1][1];
        if constexpr (M == 23) *(LAS f32x4*)(snd + 7 * 1024) = acc[7][1];
        if constexpr (M >= BAR_L) {
            constexpr int n0 = (M - BAR_L) * 8 / (32 - BAR_L), n1 = (M + 1 - BAR_L) * 8 / (32 - BAR_L);
#pragma unroll
            for (int j = n0; j < n1; ++j) rc[j] = *(const LAS f32x4*)(rcv + j * 1024);
        }
        __builtin_amdgcn_sched_barrier(0);
        stage_last<M + 1>(acc, xa, wc, rc, p, snd, rcv);
    }
}

__global__ void __launch_bounds__(512, 2)
conv_stem_fused(const u16* __restrict__ x, const u16* __restrict__ w, const u16* __restrict__ bias,
                const u16* __restrict__ gamma, const u16* __restrict__ beta, u16* __restrict__ out,
                unsigned char* __restrict__ ws) {
    __shared__ __attribute__((aligned(16))) unsigned char smem[LDS_BYTES];
    LAS unsigned char* L = (LAS unsigned char*)smem;
    const unsigned lbase = (unsigned)(size_t)L;

    const int tid = threadIdx.x, lane = tid & 63;
    const int wave = __builtin_amdgcn_readfirstlane(tid >> 6);
    const int bid = blockIdx.x, xcd = bid & 7, idx = bid >> 3;
    const int R = 8 * (xcd >> 1) + (idx & 7), ct = 4 * (xcd & 1) + (idx >> 3);
    const int b = R >> 4, rt = R & 15, grp = b * 8 + ct;
    const int l0 = rt * TS, nv = rt == 15 ? L_ - 15 * TS : TS;
    u64* const box = (u64*)(ws + WS_BOX) + grp * 1024;
    u64* const flags = (u64*)(ws + WS_FLAG) + grp * 16;
    u64* const epoch = (u64*)(ws + WS_EPOCH + grp * 256);
    u64* const hint = (u64*)(ws + WS_HINT) + grp * 256;
    STAMP(0);

    const int c16 = lane & 15, g4 = lane >> 4;
    const int r8 = lane >> 3, c8 = lane & 7;
    f32x4 v[2][2];
    const LAS unsigned char* const yr = L + YB + (16 * wave + r8) * 128 + ((c8 ^ (wave & 2)) << 4);

    if (wave >= 4) {
        const int lw = wave - 4;
        const __amdgpu_buffer_rsrc_t sx = __builtin_amdgcn_make_buffer_rsrc((void*)x, 0, 2 * T_ * D_ * 2, 0x00020000);
        const __amdgpu_buffer_rsrc_t sw = __builtin_amdgcn_make_buffer_rsrc((void*)w, 0, 5 * D_ * D_ * 2, 0x00020000);
        const u16 gam16 = gamma[ct * 64 + 16 * lw + c16], bet16 = beta[ct * 64 + 16 * lw + c16];
        unsigned vx[4];
#pragma unroll
        for (int n = 0; n < 4; ++n) {
            const int p = min(l0 + 64 * (lw & 1) + 8 * r8 + 2 * n + (lw >> 1), T_ / 2 - 1);
            vx[n] = p * 2048 + ((c8 ^ (r8 & 6)) << 4);
        }
        const unsigned vw = r8 * 1024 + ((c8 ^ (r8 & 2) ^ ((lw & 1) << 2)) << 4);
        const unsigned sx0 = b * (T_ * D_ * 2), sw0 = ct * 128 + lw * 8192;
        const unsigned mx0 = lbase + XS + lw * 1024, mwe0 = lbase + WE + lw * 1024, mwo0 = lbase + WO + lw * 1024;
        const int rot = ROT_EXPR;
        auto issue_e = [&](int cc, int lo, int hi) {
            const int cr = (cc + rot) & 7;
            const unsigned so = sx0 + cr * 128, mx = mx0 + (cc & 1) * 32768, mw = mwe0 + (cc & 1) * 24576, wo = sw0 + cr * 65536;
#pragma unroll
            for (int n = 0; n < 4; ++n) if (n >= lo && n < hi) dmax(mx + n * 4096, vx[n], sx, so);
#pragma unroll
            for (int ti = 0; ti < 3; ++ti)
#pragma unroll
                for (int h = 0; h < 2; ++h) if (4 + 2 * ti + h >= lo && 4 + 2 * ti + h < hi) dmaw(mw + ti * 8192 + h * 4096, vw, sw, wo + ti * 2 * 524288 + h * 32768);
        };
        auto issue_o = [&](int cc) {
            const int cr = (cc + rot) & 7;
            const unsigned so = sx0 + cr * 128 + 1024, mx = mx0 + (cc & 1) * 32768 + 16384, mw = mwo0 + (cc & 1) * 16384, wo = sw0 + cr * 65536 + 524288;
#pragma unroll
            for (int n = 0; n < 4; ++n) dmax(mx + n * 4096, vx[n], sx, so);
#pragma unroll
            for (int ti = 0; ti < 2; ++ti)
#pragma unroll
                for (int h = 0; h < 2; ++h) dmaw(mw + ti * 8192 + h * 4096, vw, sw, wo + ti * 2 * 524288 + h * 32768);
        };
        issue_e(0, 0, 10); issue_o(0);
        STAMP(1);
        VM_BAR(8);
        STAMP(2);
        issue_e(1, 0, 10); VM_BAR(10);
        issue_o(1); VM_BAR(8);
        issue_e(2, 0, 10); issue_o(2); VM_BAR(18);
        issue_e(3, 0, SPLIT); VM_BARN(8 + SPLIT);
        for (int cc = 2; cc < 6; ++cc) {
            issue_e(cc + 1, SPLIT, 10); issue_o(cc + 1); VM_BAR(18);
            issue_e(cc + 2, 0, SPLIT); VM_BARN(8 + SPLIT);
        }
        issue_e(7, SPLIT, 10); issue_o(7); VM_BAR(18);
        VM_BAR(8);
        VM_BAR(0);
        STAMP(3);
        const u64 e1 = *(volatile LAS u64*)(L + L_EP) + 1;
        u64 fl = lane < 16 ? ld_dev(flags + lane) : e1;
        const unsigned th = (unsigned)__builtin_amdgcn_s_memrealtime();
        if (lw == 0 && lane == 0) st_dev(hint + rt * 16, (e1 << 32) | th);
        BAR();
        STAMP(4);
        for (unsigned sp = 0; __any(fl != e1) && sp < SPIN_MAX; ++sp) fl = lane < 16 ? ld_dev(flags + lane) : e1;
        if (rt == 0 && lw == 0 && lane == 0) st_dev(epoch, e1);
        volatile LAS unsigned* tpw = (volatile LAS unsigned*)(L + L_TP);
        if (lw == 0) {
            u64 h = (e1 << 32) | th;
            for (unsigned sp = 0; sp < SPIN_MAX; ++sp) {
                if (lane < 16) h = ld_dev(hint + lane * 16);
                if (!__any((unsigned)(h >> 32) != (unsigned)e1)) break;
            }
            int d = (unsigned)(h >> 32) == (unsigned)e1 ? (int)((unsigned)h - th) : 0;
#pragma unroll
            for (int o = 1; o < 16; o <<= 1) d = max(d, __shfl_xor(d, o));
            d = min(max(d, 0), 1000);
            if (lane == 0) tpw[4] = 0x80000000u | (unsigned)d;
        }
        unsigned tp = 0, dl = 0;
        for (unsigned sp = 0; sp < SPIN_MAX * 16; ++sp) {
            tp = tpw[lw]; dl = tpw[4];
            if (tp != 0 && dl != 0) break;
            __builtin_amdgcn_s_sleep(1);
        }
        const unsigned tstart = tp + (dl & 0xFFFFu) + MARGIN;
        while ((int)((unsigned)__builtin_amdgcn_s_memrealtime() - tstart) < 0) {}
        const u64* src = box + g4 * 256 + 16 * lw + c16;
        u64 g[4];
        STAMP(8);
#pragma unroll
        for (int i = 0; i < 4; ++i) g[i] = ld_dev(src + i * 64);
        BAR();
#pragma unroll
        for (int u = 0; u < 2; ++u)
#pragma unroll
            for (int p = 0; p < 2; ++p) v[u][p] = *(const LAS f32x4*)(yr + p * YPL + u * 1024);
        for (unsigned sp = 0; sp < SPIN_MAX; ++sp) {
            bool bad = false;
#pragma unroll
            for (int i = 0; i < 4; ++i) bad |= (unsigned)(g[i] >> 32) == 0xFFFFFFFFu;
            const bool more = __any(bad);
            if (!more) break;
#pragma unroll
            for (int i = 0; i < 4; ++i) g[i] = ld_dev(src + i * 64);
        }
        STAMP(5);
        float n[4], s = 0.f;
#pragma unroll
        for (int i = 0; i < 4; ++i) {
            n[i] = (float)TS - ((i == 3 && g4 == 3) ? (float)(16 * TS - L_) : 0.f);
            s += n[i] * __uint_as_float((unsigned)g[i]);
        }
        const float mean = quad_sum(s) * (1.f / L_);
        float q = 0.f;
#pragma unroll
        for (int i = 0; i < 4; ++i) { const float d = __uint_as_float((unsigned)g[i]) - mean; q += __uint_as_float((unsigned)(g[i] >> 32)) + n[i] * d * d; }
        const float sc = bf2f(gam16) * __builtin_amdgcn_rsqf(quad_sum(q) * (1.f / L_) + 1e-5f);
        if (lane < 16) {
            *(LAS float*)(L + L_AC + (16 * lw + lane) * 4) = sc;
            *(LAS float*)(L + L_AC + 256 + (16 * lw + lane) * 4) = bf2f(bet16) - mean * sc;
        }
    } else {
        const int kh = wave & 1, chh = wave >> 1;
        const float b0 = bf2f(bias[ct * 64 + 16 * wave + c16]);
        f32x4 acc[8][2];
#pragma unroll
        for (int j = 0; j < 8; ++j) {
            acc[j][0] = f32x4{b0, b0, b0, b0}; acc[j][1] = f32x4{0.f, 0.f, 0.f, 0.f};
            asm volatile("" : "+v"(acc[j][0]), "+v"(acc[j][1]));
        }
        u64 e0 = 0;
        if (wave == 0) {
            e0 = ld_dev(epoch);
            st_dev(box + rt * 64 + lane, BOX_EMPTY);
            if (lane < 5) *(volatile LAS unsigned*)(L + L_TP + lane * 4) = 0;
        }
        Ptr p;
        p.xa = lbase + XS + c16 * 128 + (((4 * kh + g4) ^ (c16 & 6)) << 4);
        p.xb = lbase + XS + (c16 + 1) * 128 + (((4 * kh + g4) ^ ((c16 + 1) & 6)) << 4);
        {
            const int q4 = c16 >> 2, p4 = lane & 3;
#pragma unroll
            for (int u = 0; u < 2; ++u) {
                const unsigned wl = kh * 4096 + (8 * g4 + q4) * 128 + (((4 * chh + 2 * (u ^ kh) + (p4 >> 1)) ^ (q4 & 2) ^ ((g4 & 1) << 2)) << 4) + 8 * (p4 & 1);
                p.we[u] = lbase + WE + wl; p.wo[u] = lbase + WO + wl;
            }
        }
        asm volatile("" : "+v"(p.xa), "+v"(p.xb), "+v"(p.we[0]), "+v"(p.we[1]), "+v"(p.wo[0]), "+v"(p.wo[1]));
        LAS unsigned char* const snd = L + EX + wave * 8192 + lane * 16;
        bf16x8 xa[10], we[3][2], wo[3][2];
        BAR();
        STAMP(1);
#define SB() __builtin_amdgcn_sched_barrier(0)
        we[0][1] = rd_w<false>(p, 0, 1); SB(); we[0][0] = rd_w<false>(p, 0, 0); SB();
        xa[0] = rd_x<false>(p, 0); SB(); xa[1] = rd_x<false>(p, 1); SB();
        we[1][1] = rd_w<false>(p, 1, 1); SB(); we[1][0] = rd_w<false>(p, 1, 0); SB();
        xa[2] = rd_x<false>(p, 2); SB();
        we[2][1] = rd_w<false>(p, 2, 1); SB(); we[2][0] = rd_w<false>(p, 2, 0); SB();
        xa[3] = rd_x<false>(p, 3); SB();
#undef SB
        f32x4 y[8];
        const LAS unsigned char* const rcv = L + EX + (wave ^ 1) * 8192 + lane * 16;
        int dx = 32768, de = 24576, dq = 16384;
        const int trig = wave == 0 ? 1 : -1;
#pragma unroll 1
        for (int it = 0; it < 7; ++it) {
            stage_ops<false, 0>(acc, xa, we, wo, p, dx, de, dq);
            stage_ops<true, 0>(acc, xa, wo, we, p, dx, de, dq);
            int itv = it;
            asm volatile("" : "+s"(itv));
            if (itv == trig) {
                asm volatile("s_waitcnt vmcnt(0)" : "+v"(e0) :: "memory");
                if (lane == 0) { *(volatile LAS u64*)(L + L_EP) = e0; st_dev(flags + rt, e0 + 1); }
            }
        }
        stage_ops<false, 0>(acc, xa, we, wo, p, dx, de, dq);
        stage_last<0>(acc, xa, wo, y, p, snd, rcv);
        asm volatile("s_nop 7" : "+v"(acc[0][0]), "+v"(acc[1][0]), "+v"(acc[2][0]), "+v"(acc[3][0]),
                                 "+v"(acc[4][0]), "+v"(acc[5][0]), "+v"(acc[6][0]), "+v"(acc[7][0]));
        STAMP(2);
#pragma unroll
        for (int j = 0; j < 8; ++j) y[j] += acc[j][0];
        STAMP(3);
        {
            const bool k5 = g4 == 3, k3 = k5 && rt == 15;
            f32x2 s0 = {0.f, 0.f}, s1 = {0.f, 0.f};
#pragma unroll
            for (int j = 0; j < 8; ++j) {
                const float y3 = (j >= 5 ? k5 : j >= 3 ? k3 : false) ? 0.f : y[j][3];
                s0 += f32x2{y[j][0], y[j][1]}; s1 += f32x2{y[j][2], y3};
            }
            s0 += s1;
            const float m = quad_sum(s0[0] + s0[1]) * (rt == 15 ? 1.f / (L_ - 15 * TS) : 1.f / TS);
            const f32x2 mm = {m, m};
            f32x2 q0 = {0.f, 0.f}, q1 = {0.f, 0.f};
#pragma unroll
            for (int j = 0; j < 8; ++j) {
                const float y3 = (j >= 5 ? k5 : j >= 3 ? k3 : false) ? m : y[j][3];
                const f32x2 d0 = f32x2{y[j][0], y[j][1]} - mm, d1 = f32x2{y[j][2], y3} - mm;
                q0 += d0 * d0; q1 += d1 * d1;
            }
            q0 += q1;
            const float m2 = quad_sum(q0[0] + q0[1]);
            unsigned m2u = __float_as_uint(m2);
            if (m2u == 0xFFFFFFFFu) m2u = 0x7FFFFFFFu;
            if (lane < 16) st_dev(box + rt * 64 + 16 * wave + lane, ((u64)m2u << 32) | __float_as_uint(m));
            const unsigned tp = (unsigned)__builtin_amdgcn_s_memrealtime() | 1u;
            if (lane == 0) *(volatile LAS unsigned*)(L + L_TP + wave * 4) = tp;
        }
        STAMP(4);
        {
            const int word = (8 * wave + 4 * (c16 >> 3) + (c16 & 3)) ^ ((g4 & 1) << 3);
            LAS unsigned char* yp = L + YB + ((c16 >> 2) & 1) * YPL + 32 * g4 * 128 + word * 4;
#pragma unroll
            for (int j = 0; j < 8; ++j)
#pragma unroll
                for (int i = 0; i < 4; ++i) *(LAS float*)(yp + (8 * i + j) * 128) = y[j][i];
        }
        LGKM_BAR();
#pragma unroll
        for (int u = 0; u < 2; ++u)
#pragma unroll
            for (int pl = 0; pl < 2; ++pl) v[u][pl] = *(const LAS f32x4*)(yr + pl * YPL + u * 1024);
    }
    LGKM_BAR();
    STAMP(6);
    f32x4 sa[2], sc[2];
#pragma unroll
    for (int p = 0; p < 2; ++p) {
        sa[p] = *(const LAS f32x4*)(L + L_AC + c8 * 32 + p * 16);
        sc[p] = *(const LAS f32x4*)(L + L_AC + 256 + c8 * 32 + p * 16);
    }
    constexpr float K1 = -2.0f * 1.4426950408889634f * 0.7978845608028654f, K2 = K1 * 0.044715f;
#pragma unroll
    for (int u = 0; u < 2; ++u) {
        const int t = 16 * wave + 8 * u + r8;
        unsigned o[4];
#pragma unroll
        for (int h = 0; h < 4; ++h) {
            const int p = h >> 1, e = (h & 1) * 2;
            const f32x2 z = f32x2{v[u][p][e], v[u][p][e + 1]} * f32x2{sa[p][e], sa[p][e + 1]} + f32x2{sc[p][e], sc[p][e + 1]};
            const f32x2 arg = z * (z * z * K2 + K1);
            const f32x2 dn = f32x2{__builtin_amdgcn_exp2f(arg[0]), __builtin_amdgcn_exp2f(arg[1])} + 1.f;
            const f32x2 r = z * f32x2{__builtin_amdgcn_rcpf(dn[0]), __builtin_amdgcn_rcpf(dn[1])};
            o[h] = __builtin_bit_cast(unsigned, bf16x2{(__bf16)r[0], (__bf16)r[1]});
        }
        if (t < nv) {
            const u32x4 ov = u32x4{o[0], o[1], o[2], o[3]};
            u16* op = out + ((size_t)(b * L_ + l0 + t) * D_ + ct * 64 + c8 * 8);
            asm volatile("global_store_dwordx4 %0, %1, off " STPOL :: "v"(op), "v"(ov) : "memory");
        }
    }
    STAMP(7);
}

extern "C" void kernel_launch(void* const* d_in, const int* in_sizes, int n_in, void* d_out, int out_size,
                              void* d_ws, size_t ws_size, hipStream_t stream) {
    conv_stem_fused<<<256, 512, 0, stream>>>((const u16*)d_in[0], (const u16*)d_in[1], (const u16*)d_in[2],
                                             (const u16*)d_in[3], (const u16*)d_in[4], (u16*)d_out, (unsigned char*)d_ws);
}
```

```cpp
#define STAMPS
#include <stdio.h>
#include <hip/hip_runtime.h>
#include <stdint.h>

typedef unsigned short u16;
typedef unsigned long long u64;
typedef __attribute__((ext_vector_type(8))) __bf16 bf16x8;
typedef __attribute__((ext_vector_type(2))) __bf16 bf16x2;
typedef __attribute__((ext_vector_type(4))) short s16x4;
typedef __attribute__((ext_vector_type(8))) short s16x8;
typedef __attribute__((ext_vector_type(16))) float f32x16;
typedef __attribute__((ext_vector_type(4))) float f32x4;
typedef __attribute__((ext_vector_type(2))) float f32x2;
typedef __attribute__((ext_vector_type(4))) unsigned u32x4;
#define LAS __attribute__((address_space(3)))
#define DI __device__ __forceinline__

constexpr int T_ = 4000, D_ = 512, L_ = 1998, TS = 125;

constexpr unsigned XS = 0;
constexpr unsigned WE = 65536;
constexpr unsigned WO = WE + 49152;
constexpr unsigned MISC = WO + 32768;
constexpr unsigned L_EP = MISC;
constexpr unsigned L_AC = MISC + 64;
constexpr unsigned L_TP = L_AC + 512;
constexpr unsigned LDS_BYTES = L_TP + 64;
constexpr unsigned EX = 0;
constexpr unsigned YB = 32768;
constexpr unsigned YPL = 16384 + 64;

constexpr size_t WS_EPOCH = 0;
constexpr size_t WS_FLAG = 4096;
constexpr size_t WS_HINT = 8192;
constexpr size_t WS_BOX = 65536;
constexpr size_t WS_DBG = 64u << 20;
#define STAMP(i) do { if (lane == 0) { ((u64*)(ws + WS_DBG))[(blockIdx.x * 8 + wave) * 16 + (i)] = __builtin_amdgcn_s_memrealtime(); \
    ((u64*)(ws + WS_DBG + (1u << 20)))[(blockIdx.x * 8 + wave) * 16 + (i)] = __builtin_amdgcn_s_memtime(); } } while (0)

#define ROT_EXPR ((R + 2 * ct) & 7)
#define PER_A 2
#define PER_B 2
#define SPLIT 7
#define MARGIN 15
#define PRO 1
#define STPOL "sc1"
constexpr u64 BOX_EMPTY = 0xFFFFFFFFFFFFFFFFull;
constexpr unsigned SPIN_MAX = 1u << 16;

DI float bf2f(u16 v) { return __uint_as_float(((unsigned)v) << 16); }
DI u64 ld_dev(const u64* p) { return __hip_atomic_load(p, __ATOMIC_RELAXED, __HIP_MEMORY_SCOPE_AGENT); }
DI void st_dev(u64* p, u64 v) { __hip_atomic_store(p, v, __ATOMIC_RELAXED, __HIP_MEMORY_SCOPE_AGENT); }
DI float half_sum(float v) {
    const unsigned u = __float_as_uint(v);
    const auto r = __builtin_amdgcn_permlane32_swap(u, u, false, false);
    return __uint_as_float(r[0]) + __uint_as_float(r[1]);
}
#define BAR() asm volatile("s_barrier" ::: "memory")
#define LGKM_BAR() asm volatile("s_waitcnt lgkmcnt(0)\n\ts_barrier" ::: "memory")
#define VM_BARN(n) asm volatile("s_waitcnt vmcnt(%0)\n\ts_barrier" :: "n"(n) : "memory")
#define VM_BAR(n) asm volatile("s_waitcnt vmcnt(" #n ")\n\ts_barrier" ::: "memory")

#define XPOL ""
#define WPOL ""
DI void dmax(unsigned ldsaddr, unsigned voff, __amdgpu_buffer_rsrc_t srd, unsigned soff) {
    asm volatile("s_mov_b32 m0, %0\n\ts_nop 0\n\tbuffer_load_dwordx4 %1, %2, %3 offen " XPOL " lds"
                 :: "s"(ldsaddr), "v"(voff), "s"(srd), "s"(soff) : "m0", "memory");
}
DI void dmaw(unsigned ldsaddr, unsigned voff, __amdgpu_buffer_rsrc_t srd, unsigned soff) {
    asm volatile("s_mov_b32 m0, %0\n\ts_nop 0\n\tbuffer_load_dwordx4 %1, %2, %3 offen " WPOL " lds"
                 :: "s"(ldsaddr), "v"(voff), "s"(srd), "s"(soff) : "m0", "memory");
}

DI float quad_sum(float v) {
    const unsigned u = __float_as_uint(v);
    const auto r = __builtin_amdgcn_permlane16_swap(u, u, false, false);
    return half_sum(__uint_as_float(r[0]) + __uint_as_float(r[1]));
}
DI bf16x8 ld_a(const LAS unsigned char* p) { return *(const LAS bf16x8*)p; }
DI bf16x8 ld_b(const LAS unsigned char* p) {
    const s16x4 lo = __builtin_amdgcn_ds_read_tr16_b64_v4i16((LAS s16x4*)p);
    const s16x4 hi = __builtin_amdgcn_ds_read_tr16_b64_v4i16((LAS s16x4*)(p + 512));
    const s16x8 r = __builtin_shufflevector(lo, hi, 0, 1, 2, 3, 4, 5, 6, 7);
    return __builtin_bit_cast(bf16x8, r);
}

struct Op { int a, s, u; };
constexpr Op op_of(int nt, int m) {
    int k = 0;
    for (int a = 0; a < 7 + nt; ++a)
        for (int s = 0; s < nt; ++s)
            if (a - s >= 0 && a - s < 8)
                for (int u = 1; u >= 0; --u) { if (k == m) return Op{a, s, u}; ++k; }
    return Op{-1, -1, -1};
}
#define BARL 27
constexpr int BAR_E = 36, BAR_O = 18, BAR_L = BARL;
constexpr int rd_even(int m) {
    switch (m) {
        case 2: return 4; case 7: return 5; case 12: return 6; case 17: return 7; case 21: return 8; case 25: return 9;
        case 36: return 21; case 37: return 20; case 38: return 10; case 39: return 11; case 40: return 23; case 41: return 22;
        case 42: return 12; case 43: return 13; case 45: return 14;
        default: return -1;
    }
}
constexpr int rd_odd(int m) {
    switch (m) {
        case 0: return 5; case 2: return 6; case 4: return 7; case 6: return 8;
        case 18: return 21; case 19: return 20; case 20: return 10; case 21: return 11; case 22: return 23; case 23: return 22;
        case 24: return 12; case 25: return 25; case 26: return 24; case 28: return 13;
        default: return -1;
    }
}
struct Ptr { unsigned xa, xb, we[2], wo[2]; };
DI const LAS unsigned char* lds_at(unsigned a) { return (const LAS unsigned char*)(size_t)a; }
template <bool ODD> DI bf16x8 rd_x(const Ptr& p, int a) {
    return a < 8 ? ld_a(lds_at(p.xa) + (ODD ? 16384 : 0) + a * 2048) : ld_a(lds_at(p.xb) + (ODD ? 16384 : 0) + (a - 8) * 2048);
}
template <bool ODD> DI bf16x8 rd_w(const Ptr& p, int s, int u) { return ld_b(lds_at(ODD ? p.wo[u] : p.we[u]) + s * 8192); }
DI void mma(f32x4& c, const bf16x8& a, const bf16x8& b) {
    asm volatile("v_mfma_f32_16x16x32_bf16 %0, %1, %2, %0" : "+v"(c) : "v"(a), "v"(b));
}
DI void flip(unsigned& a, int d) { asm volatile("v_add_u32 %0, %1, %0" : "+v"(a) : "s"(d)); }
DI void negate(int& d) { asm volatile("s_sub_i32 %0, 0, %0" : "+s"(d) :: "scc"); }
template <bool ODD, int M>
DI void stage_ops(f32x4 (&acc)[8][2], bf16x8 (&xa)[10], const bf16x8 (&wc)[3][2], bf16x8 (&wn)[3][2], Ptr& p, int& dx, int& de, int& dq) {
    constexpr int NT = ODD ? 2 : 3, NM = 16 * NT;
    if constexpr (M < NM) {
        if constexpr (M == (ODD ? BAR_O : BAR_E)) { LGKM_BAR(); __builtin_amdgcn_sched_barrier(0); }
        constexpr Op o = op_of(NT, M);
        mma(acc[o.a - o.s][o.u], xa[o.a], wc[o.s][o.u]);
        __builtin_amdgcn_sched_barrier(0);
        constexpr int rd = ODD ? rd_odd(M) : rd_even(M);
        if constexpr (rd >= 0 && rd < 10) xa[rd] = rd_x<ODD>(p, rd);
        if constexpr (rd >= 10 && rd < 20) xa[rd - 10] = rd_x<!ODD>(p, rd - 10);
        if constexpr (rd >= 20) wn[(rd - 20) >> 1][rd & 1] = rd_w<!ODD>(p, (rd - 20) >> 1, rd & 1);
        if constexpr (ODD && M == 8) flip(p.xa, dx);
        if constexpr (ODD && M == 9) flip(p.xb, dx);
        if constexpr (ODD && M == 10) flip(p.we[0], de);
        if constexpr (ODD && M == 11) flip(p.we[1], de);
        if constexpr (ODD && M == 12) flip(p.wo[0], dq);
        if constexpr (ODD && M == 13) flip(p.wo[1], dq);
        if constexpr (ODD && M == 14) { negate(dx); negate(de); }
        if constexpr (ODD && M == 15) negate(dq);
        __builtin_amdgcn_sched_barrier(0);
        stage_ops<ODD, M + 1>(acc, xa, wc, wn, p, dx, de, dq);
    }
}
template <int M>
DI void stage_last(f32x4 (&acc)[8][2], bf16x8 (&xa)[10], const bf16x8 (&wc)[3][2], f32x4 (&rc)[8], const Ptr& p,
                   LAS unsigned char* snd, const LAS unsigned char* rcv) {
    if constexpr (M < 32) {
        if constexpr (M == BAR_L) { LGKM_BAR(); __builtin_amdgcn_sched_barrier(0); }
        constexpr Op o = op_of(2, 2 * (M & 15));
        constexpr int u = M < 16 ? 1 : 0;
        mma(acc[o.a - o.s][u], xa[o.a], wc[o.s][u]);
        __builtin_amdgcn_sched_barrier(0);
        if constexpr (M < 4) xa[5 + M] = rd_x<true>(p, 5 + M);
        if constexpr (M >= 10 && M <= 22 && (M & 1) == 0) *(LAS f32x4*)(snd + ((M - 10) >> 1) * 1024) = acc[(M - 10) >> 1][1];
        if constexpr (M == 23) *(LAS f32x4*)(snd + 7 * 1024) = acc[7][1];
        if constexpr (M >= BAR_L) {
            constexpr int n0 = (M - BAR_L) * 8 / (32 - BAR_L), n1 = (M + 1 - BAR_L) * 8 / (32 - BAR_L);
#pragma unroll
            for (int j = n0; j < n1; ++j) rc[j] = *(const LAS f32x4*)(rcv + j * 1024);
        }
        __builtin_amdgcn_sched_barrier(0);
        stage_last<M + 1>(acc, xa, wc, rc, p, snd, rcv);
    }
}

__global__ void __launch_bounds__(512, 2)
conv_stem_fused(const u16* __restrict__ x, const u16* __restrict__ w, const u16* __restrict__ bias,
                const u16* __restrict__ gamma, const u16* __restrict__ beta, u16* __restrict__ out,
                unsigned char* __restrict__ ws) {
    __shared__ __attribute__((aligned(16))) unsigned char smem[LDS_BYTES];
    const u64 tpre = __builtin_amdgcn_s_memrealtime();
    __builtin_amdgcn_sched_barrier(0);
    LAS unsigned char* L = (LAS unsigned char*)smem;
    const unsigned lbase = (unsigned)(size_t)L;

    const int tid = threadIdx.x, lane = tid & 63;
    const int wave = __builtin_amdgcn_readfirstlane(tid >> 6);
    const int bid = blockIdx.x, xcd = bid & 7, idx = bid >> 3;
    const int R = 8 * (xcd >> 1) + (idx & 7), ct = 4 * (xcd & 1) + (idx >> 3);
    const int b = R >> 4, rt = R & 15, grp = b * 8 + ct;
    const int l0 = rt * TS, nv = rt == 15 ? L_ - 15 * TS : TS;
    u64* const box = (u64*)(ws + WS_BOX) + grp * 1024;
    u64* const flags = (u64*)(ws + WS_FLAG) + grp * 16;
    u64* const epoch = (u64*)(ws + WS_EPOCH + grp * 256);
    u64* const hint = (u64*)(ws + WS_HINT) + grp * 256;
    STAMP(0);
    if ((threadIdx.x & 63) == 0) ((u64*)(ws + WS_DBG))[(blockIdx.x * 8 + wave) * 16 + 10] = tpre;

    const int c16 = lane & 15, g4 = lane >> 4;
    const int r8 = lane >> 3, c8 = lane & 7;
    f32x4 v[2][2];
    const LAS unsigned char* const yr = L + YB + (16 * wave + r8) * 128 + ((c8 ^ (wave & 2)) << 4);

    if (wave >= 4) {
        const int lw = wave - 4;
        const __amdgpu_buffer_rsrc_t sx = __builtin_amdgcn_make_buffer_rsrc((void*)x, 0, 2 * T_ * D_ * 2, 0x00020000);
        const __amdgpu_buffer_rsrc_t sw = __builtin_amdgcn_make_buffer_rsrc((void*)w, 0, 5 * D_ * D_ * 2, 0x00020000);
        const u16 gam16 = gamma[ct * 64 + 16 * lw + c16], bet16 = beta[ct * 64 + 16 * lw + c16];
        unsigned vx[4];
#pragma unroll
        for (int n = 0; n < 4; ++n) {
            const int p = min(l0 + 64 * (lw & 1) + 8 * r8 + 2 * n + (lw >> 1), T_ / 2 - 1);
            vx[n] = p * 2048 + ((c8 ^ (r8 & 6)) << 4);
        }
        const unsigned vw = r8 * 1024 + ((c8 ^ (r8 & 2) ^ ((lw & 1) << 2)) << 4);
        const unsigned sx0 = b * (T_ * D_ * 2), sw0 = ct * 128 + lw * 8192;
        const unsigned mx0 = lbase + XS + lw * 1024, mwe0 = lbase + WE + lw * 1024, mwo0 = lbase + WO + lw * 1024;
        const int rot = ROT_EXPR;
        auto issue_e = [&](int cc, int lo, int hi) {
            const int cr = (cc + rot) & 7;
            const unsigned so = sx0 + cr * 128, mx = mx0 + (cc & 1) * 32768, mw = mwe0 + (cc & 1) * 24576, wo = sw0 + cr * 65536;
#pragma unroll
            for (int n = 0; n < 4; ++n) if (n >= lo && n < hi) dmax(mx + n * 4096, vx[n], sx, so);
#pragma unroll
            for (int ti = 0; ti < 3; ++ti)
#pragma unroll
                for (int h = 0; h < 2; ++h) if (4 + 2 * ti + h >= lo && 4 + 2 * ti + h < hi) dmaw(mw + ti * 8192 + h * 4096, vw, sw, wo + ti * 2 * 524288 + h * 32768);
        };
        auto issue_o = [&](int cc) {
            const int cr = (cc + rot) & 7;
            const unsigned so = sx0 + cr * 128 + 1024, mx = mx0 + (cc & 1) * 32768 + 16384, mw = mwo0 + (cc & 1) * 16384, wo = sw0 + cr * 65536 + 524288;
#pragma unroll
            for (int n = 0; n < 4; ++n) dmax(mx + n * 4096, vx[n], sx, so);
#pragma unroll
            for (int ti = 0; ti < 2; ++ti)
#pragma unroll
                for (int h = 0; h < 2; ++h) dmaw(mw + ti * 8192 + h * 4096, vw, sw, wo + ti * 2 * 524288 + h * 32768);
        };
        issue_e(0, 0, 10); issue_o(0);
        STAMP(1);
        VM_BAR(8);
        STAMP(2);
        issue_e(1, 0, 10); VM_BAR(10);
        issue_o(1); VM_BAR(8);
        issue_e(2, 0, 10); issue_o(2); VM_BAR(18);
        issue_e(3, 0, SPLIT); VM_BARN(8 + SPLIT);
        for (int cc = 2; cc < 6; ++cc) {
            issue_e(cc + 1, SPLIT, 10); issue_o(cc + 1); VM_BAR(18);
            issue_e(cc + 2, 0, SPLIT); VM_BARN(8 + SPLIT);
        }
        issue_e(7, SPLIT, 10); issue_o(7); VM_BAR(18);
        VM_BAR(8);
        VM_BAR(0);
        STAMP(3);
        const u64 e1 = *(volatile LAS u64*)(L + L_EP) + 1;
        u64 fl = lane < 16 ? ld_dev(flags + lane) : e1;
        const unsigned th = (unsigned)__builtin_amdgcn_s_memrealtime();
        if (lw == 0 && lane == 0) st_dev(hint + rt * 16, (e1 << 32) | th);
        BAR();
        STAMP(4);
        for (unsigned sp = 0; __any(fl != e1) && sp < SPIN_MAX; ++sp) fl = lane < 16 ? ld_dev(flags + lane) : e1;
        if (rt == 0 && lw == 0 && lane == 0) st_dev(epoch, e1);
        volatile LAS unsigned* tpw = (volatile LAS unsigned*)(L + L_TP);
        if (lw == 0) {
            u64 h = (e1 << 32) | th;
            for (unsigned sp = 0; sp < SPIN_MAX; ++sp) {
                if (lane < 16) h = ld_dev(hint + lane * 16);
                if (!__any((unsigned)(h >> 32) != (unsigned)e1)) break;
            }
            int d = (unsigned)(h >> 32) == (unsigned)e1 ? (int)((unsigned)h - th) : 0;
#pragma unroll
            for (int o = 1; o < 16; o <<= 1) d = max(d, __shfl_xor(d, o));
            d = min(max(d, 0), 1000);
            if (lane == 0) tpw[4] = 0x80000000u | (unsigned)d;
        }
        unsigned tp = 0, dl = 0;
        for (unsigned sp = 0; sp < SPIN_MAX * 16; ++sp) {
            tp = tpw[lw]; dl = tpw[4];
            if (tp != 0 && dl != 0) break;
            __builtin_amdgcn_s_sleep(1);
        }
        const unsigned tstart = tp + (dl & 0xFFFFu) + MARGIN;
        while ((int)((unsigned)__builtin_amdgcn_s_memrealtime() - tstart) < 0) {}
        const u64* src = box + g4 * 256 + 16 * lw + c16;
        u64 g[4];
        STAMP(8);
#pragma unroll
        for (int i = 0; i < 4; ++i) g[i] = ld_dev(src + i * 64);
        BAR();
#pragma unroll
        for (int u = 0; u < 2; ++u)
#pragma unroll
            for (int p = 0; p < 2; ++p) v[u][p] = *(const LAS f32x4*)(yr + p * YPL + u * 1024);
        unsigned npass = 0;
        u64 tl = __builtin_amdgcn_s_memrealtime(), tlast = 0;
        for (unsigned sp = 0; sp < SPIN_MAX; ++sp) {
            bool bad = false;
#pragma unroll
            for (int i = 0; i < 4; ++i) bad |= (unsigned)(g[i] >> 32) == 0xFFFFFFFFu;
            const bool more = __any(bad);
            const u64 tn = __builtin_amdgcn_s_memrealtime(); tlast = tn - tl; tl = tn;
            if (lane == 0 && npass < 12) ((u64*)(ws + WS_DBG + (3u << 20)))[(blockIdx.x * 8 + wave) * 16 + npass] = tn;
            ++npass;
            if (!more) break;
#pragma unroll
            for (int i = 0; i < 4; ++i) g[i] = ld_dev(src + i * 64);
        }
        STAMP(5);
        if (lane == 0) { ((u64*)(ws + WS_DBG + (2u << 20)))[(blockIdx.x * 8 + wave) * 2] = npass; ((u64*)(ws + WS_DBG + (2u << 20)))[(blockIdx.x * 8 + wave) * 2 + 1] = tlast; }
        float n[4], s = 0.f;
#pragma unroll
        for (int i = 0; i < 4; ++i) {
            n[i] = (float)TS - ((i == 3 && g4 == 3) ? (float)(16 * TS - L_) : 0.f);
            s += n[i] * __uint_as_float((unsigned)g[i]);
        }
        const float mean = quad_sum(s) * (1.f / L_);
        float q = 0.f;
#pragma unroll
        for (int i = 0; i < 4; ++i) { const float d = __uint_as_float((unsigned)g[i]) - mean; q += __uint_as_float((unsigned)(g[i] >> 32)) + n[i] * d * d; }
        const float sc = bf2f(gam16) * __builtin_amdgcn_rsqf(quad_sum(q) * (1.f / L_) + 1e-5f);
        if (lane < 16) {
            *(LAS float*)(L + L_AC + (16 * lw + lane) * 4) = sc;
            *(LAS float*)(L + L_AC + 256 + (16 * lw + lane) * 4) = bf2f(bet16) - mean * sc;
        }
    } else {
        const int kh = wave & 1, chh = wave >> 1;
        const float b0 = bf2f(bias[ct * 64 + 16 * wave + c16]);
        f32x4 acc[8][2];
#pragma unroll
        for (int j = 0; j < 8; ++j) {
            acc[j][0] = f32x4{b0, b0, b0, b0}; acc[j][1] = f32x4{0.f, 0.f, 0.f, 0.f};
            asm volatile("" : "+v"(acc[j][0]), "+v"(acc[j][1]));
        }
        u64 e0 = 0;
        if (wave == 0) {
            e0 = ld_dev(epoch);
            st_dev(box + rt * 64 + lane, BOX_EMPTY);
            if (lane < 5) *(volatile LAS unsigned*)(L + L_TP + lane * 4) = 0;
        }
        Ptr p;
        p.xa = lbase + XS + c16 * 128 + (((4 * kh + g4) ^ (c16 & 6)) << 4);
        p.xb = lbase + XS + (c16 + 1) * 128 + (((4 * kh + g4) ^ ((c16 + 1) & 6)) << 4);
        {
            const int q4 = c16 >> 2, p4 = lane & 3;
#pragma unroll
            for (int u = 0; u < 2; ++u) {
                const unsigned wl = kh * 4096 + (8 * g4 + q4) * 128 + (((4 * chh + 2 * (u ^ kh) + (p4 >> 1)) ^ (q4 & 2) ^ ((g4 & 1) << 2)) << 4) + 8 * (p4 & 1);
                p.we[u] = lbase + WE + wl; p.wo[u] = lbase + WO + wl;
            }
        }
        asm volatile("" : "+v"(p.xa), "+v"(p.xb), "+v"(p.we[0]), "+v"(p.we[1]), "+v"(p.wo[0]), "+v"(p.wo[1]));
        LAS unsigned char* const snd = L + EX + wave * 8192 + lane * 16;
        bf16x8 xa[10], we[3][2], wo[3][2];
        BAR();
        STAMP(1);
#define SB() __builtin_amdgcn_sched_barrier(0)
        we[0][1] = rd_w<false>(p, 0, 1); SB(); we[0][0] = rd_w<false>(p, 0, 0); SB();
        xa[0] = rd_x<false>(p, 0); SB(); xa[1] = rd_x<false>(p, 1); SB();
        we[1][1] = rd_w<false>(p, 1, 1); SB(); we[1][0] = rd_w<false>(p, 1, 0); SB();
        xa[2] = rd_x<false>(p, 2); SB();
        we[2][1] = rd_w<false>(p, 2, 1); SB(); we[2][0] = rd_w<false>(p, 2, 0); SB();
        xa[3] = rd_x<false>(p, 3); SB();
#undef SB
        f32x4 y[8];
        const LAS unsigned char* const rcv = L + EX + (wave ^ 1) * 8192 + lane * 16;
        int dx = 32768, de = 24576, dq = 16384;
        const int trig = wave == 0 ? 1 : -1;
#pragma unroll 1
        for (int it = 0; it < 7; ++it) {
            stage_ops<false, 0>(acc, xa, we, wo, p, dx, de, dq);
            stage_ops<true, 0>(acc, xa, wo, we, p, dx, de, dq);
            int itv = it;
            asm volatile("" : "+s"(itv));
            if (itv == trig) {
                asm volatile("s_waitcnt vmcnt(0)" : "+v"(e0) :: "memory");
                if (lane == 0) { *(volatile LAS u64*)(L + L_EP) = e0; st_dev(flags + rt, e0 + 1); }
            }
        }
        stage_ops<false, 0>(acc, xa, we, wo, p, dx, de, dq);
        stage_last<0>(acc, xa, wo, y, p, snd, rcv);
        asm volatile("s_nop 7" : "+v"(acc[0][0]), "+v"(acc[1][0]), "+v"(acc[2][0]), "+v"(acc[3][0]),
                                 "+v"(acc[4][0]), "+v"(acc[5][0]), "+v"(acc[6][0]), "+v"(acc[7][0]));
        STAMP(2);
#pragma unroll
        for (int j = 0; j < 8; ++j) y[j] += acc[j][0];
        STAMP(3);
        {
            const bool k5 = g4 == 3, k3 = k5 && rt == 15;
            f32x2 s0 = {0.f, 0.f}, s1 = {0.f, 0.f};
#pragma unroll
            for (int j = 0; j < 8; ++j) {
                const float y3 = (j >= 5 ? k5 : j >= 3 ? k3 : false) ? 0.f : y[j][3];
                s0 += f32x2{y[j][0], y[j][1]}; s1 += f32x2{y[j][2], y3};
            }
            s0 += s1;
            const float m = quad_sum(s0[0] + s0[1]) * (rt == 15 ? 1.f / (L_ - 15 * TS) : 1.f / TS);
            const f32x2 mm = {m, m};
            f32x2 q0 = {0.f, 0.f}, q1 = {0.f, 0.f};
#pragma unroll
            for (int j = 0; j < 8; ++j) {
                const float y3 = (j >= 5 ? k5 : j >= 3 ? k3 : false) ? m : y[j][3];
                const f32x2 d0 = f32x2{y[j][0], y[j][1]} - mm, d1 = f32x2{y[j][2], y3} - mm;
                q0 += d0 * d0; q1 += d1 * d1;
            }
            q0 += q1;
            const float m2 = quad_sum(q0[0] + q0[1]);
            unsigned m2u = __float_as_uint(m2);
            if (m2u == 0xFFFFFFFFu) m2u = 0x7FFFFFFFu;
            if (lane < 16) st_dev(box + rt * 64 + 16 * wave + lane, ((u64)m2u << 32) | __float_as_uint(m));
            const unsigned tp = (unsigned)__builtin_amdgcn_s_memrealtime() | 1u;
            if (lane == 0) *(volatile LAS unsigned*)(L + L_TP + wave * 4) = tp;
        }
        STAMP(4);
        {
            const int word = (8 * wave + 4 * (c16 >> 3) + (c16 & 3)) ^ ((g4 & 1) << 3);
            LAS unsigned char* yp = L + YB + ((c16 >> 2) & 1) * YPL + 32 * g4 * 128 + word * 4;
#pragma unroll
            for (int j = 0; j < 8; ++j)
#pragma unroll
                for (int i = 0; i < 4; ++i) *(LAS float*)(yp + (8 * i + j) * 128) = y[j][i];
        }
        LGKM_BAR();
#pragma unroll
        for (int u = 0; u < 2; ++u)
#pragma unroll
            for (int pl = 0; pl < 2; ++pl) v[u][pl] = *(const LAS f32x4*)(yr + pl * YPL + u * 1024);
    }
    LGKM_BAR();
    STAMP(6);
    f32x4 sa[2], sc[2];
#pragma unroll
    for (int p = 0; p < 2; ++p) {
        sa[p] = *(const LAS f32x4*)(L + L_AC + c8 * 32 + p * 16);
        sc[p] = *(const LAS f32x4*)(L + L_AC + 256 + c8 * 32 + p * 16);
    }
    constexpr float K1 = -2.0f * 1.4426950408889634f * 0.7978845608028654f, K2 = K1 * 0.044715f;
#pragma unroll
    for (int u = 0; u < 2; ++u) {
        const int t = 16 * wave + 8 * u + r8;
        unsigned o[4];
#pragma unroll
        for (int h = 0; h < 4; ++h) {
            const int p = h >> 1, e = (h & 1) * 2;
            const f32x2 z = f32x2{v[u][p][e], v[u][p][e + 1]} * f32x2{sa[p][e], sa[p][e + 1]} + f32x2{sc[p][e], sc[p][e + 1]};
            const f32x2 arg = z * (z * z * K2 + K1);
            const f32x2 dn = f32x2{__builtin_amdgcn_exp2f(arg[0]), __builtin_amdgcn_exp2f(arg[1])} + 1.f;
            const f32x2 r = z * f32x2{__builtin_amdgcn_rcpf(dn[0]), __builtin_amdgcn_rcpf(dn[1])};
            o[h] = __builtin_bit_cast(unsigned, bf16x2{(__bf16)r[0], (__bf16)r[1]});
        }
        if (t < nv) {
            const u32x4 ov = u32x4{o[0], o[1], o[2], o[3]};
            u16* op = out + ((size_t)(b * L_ + l0 + t) * D_ + ct * 64 + c8 * 8);
            asm volatile("global_store_dwordx4 %0, %1, off " STPOL :: "v"(op), "v"(ov) : "memory");
        }
    }
    STAMP(7);
}

__global__ void dump_k(const unsigned char* ws) {
    __shared__ unsigned long long t0s;
    __shared__ float mn[24], mx[24], sm[24];
    const u64* s = (const u64*)(ws + WS_DBG);
    const int g = threadIdx.x;
    if (g == 0) t0s = ~0ull;
    if (g < 24) { mn[g] = 1e9f; mx[g] = -1e9f; sm[g] = 0.f; }
    __syncthreads();
    for (int wv = 0; wv < 8; ++wv) atomicMin(&t0s, s[(g * 8 + wv) * 16]);
    __syncthreads();
    const u64 t0 = t0s;
    for (int k = 0; k < 24; ++k) {
        const int wv = k < 11 ? 0 : 4, i = k < 11 ? k : k - 11;
        float v = k < 22 ? (float)((long long)(s[(g * 8 + wv) * 16 + i] - t0)) * 0.01f : (float)((long long)(s[(g * 8 + wv) * 16] - s[(g * 8 + wv) * 16 + 10])) * 0.01f;
        for (int r = 0; r < 256; ++r) { if (r == g) { mn[k] = fminf(mn[k], v); mx[k] = fmaxf(mx[k], v); sm[k] += v; } __syncthreads(); }
    }
    if (g == 0) {
        printf("W0 pre %.2f/%.2f/%.2f s0 %.2f/%.2f/%.2f s1 %.2f/%.2f/%.2f s2 %.2f/%.2f/%.2f s4 %.2f/%.2f/%.2f s6 %.2f/%.2f/%.2f s7 %.2f/%.2f/%.2f | W4 pre %.2f/%.2f/%.2f s1 %.2f/%.2f/%.2f s2 %.2f/%.2f/%.2f s3 %.2f/%.2f/%.2f s7 %.2f/%.2f/%.2f ka %.2f/%.2f/%.2f\n",
            mn[10], sm[10] / 256, mx[10], mn[0], sm[0] / 256, mx[0], mn[1], sm[1] / 256, mx[1], mn[2], sm[2] / 256, mx[2], mn[4], sm[4] / 256, mx[4], mn[6], sm[6] / 256, mx[6], mn[7], sm[7] / 256, mx[7],
            mn[21], sm[21] / 256, mx[21], mn[12], sm[12] / 256, mx[12], mn[13], sm[13] / 256, mx[13], mn[14], sm[14] / 256, mx[14], mn[18], sm[18] / 256, mx[18], mn[22], sm[22] / 256, mx[22]);
    }
}

extern "C" void kernel_launch(void* const* d_in, const int* in_sizes, int n_in, void* d_out, int out_size,
                              void* d_ws, size_t ws_size, hipStream_t stream) {
    conv_stem_fused<<<256, 512, 0, stream>>>((const u16*)d_in[0], (const u16*)d_in[1], (const u16*)d_in[2],
                                             (const u16*)d_in[3], (const u16*)d_in[4], (u16*)d_out, (unsigned char*)d_ws);
    dump_k<<<1, 256, 0, stream>>>((const unsigned char*)d_ws);
}
```

```cpp
#include <hip/hip_runtime.h>
#include <stdint.h>

typedef unsigned short u16;
typedef unsigned long long u64;
typedef __attribute__((ext_vector_type(8))) __bf16 bf16x8;
typedef __attribute__((ext_vector_type(2))) __bf16 bf16x2;
typedef __attribute__((ext_vector_type(4))) short s16x4;
typedef __attribute__((ext_vector_type(8))) short s16x8;
typedef __attribute__((ext_vector_type(16))) float f32x16;
typedef __attribute__((ext_vector_type(4))) float f32x4;
typedef __attribute__((ext_vector_type(2))) float f32x2;
typedef __attribute__((ext_vector_type(4))) unsigned u32x4;
#define LAS __attribute__((address_space(3)))
#define DI __device__ __forceinline__

constexpr int T_ = 4000, D_ = 512, L_ = 1998, TS = 125;

constexpr unsigned XS = 0;
constexpr unsigned WE = 65536;
constexpr unsigned WO = WE + 49152;
constexpr unsigned MISC = WO + 32768;
constexpr unsigned L_EP = MISC;
constexpr unsigned L_AC = MISC + 64;
constexpr unsigned L_TP = L_AC + 512;
constexpr unsigned LDS_BYTES = L_TP + 64;
constexpr unsigned EX = 0;
constexpr unsigned YB = 32768;
constexpr unsigned YPL = 16384 + 64;

constexpr size_t WS_EPOCH = 0;
constexpr size_t WS_FLAG = 4096;
constexpr size_t WS_HINT = 8192;
constexpr size_t WS_BOX = 65536;
#define STAMP(i) do {} while (0)

#define ROT_EXPR ((R + 2 * ct) & 7)
#define PER_A 2
#define PER_B 2
#define SPLIT 7
#define MARGIN 15
#define PRO 1
#define STPOL "sc1"
constexpr u64 BOX_EMPTY = 0xFFFFFFFFFFFFFFFFull;
constexpr unsigned SPIN_MAX = 1u << 16;

DI float bf2f(u16 v) { return __uint_as_float(((unsigned)v) << 16); }
DI u64 ld_dev(const u64* p) { return __hip_atomic_load(p, __ATOMIC_RELAXED, __HIP_MEMORY_SCOPE_AGENT); }
DI void st_dev(u64* p, u64 v) { __hip_atomic_store(p, v, __ATOMIC_RELAXED, __HIP_MEMORY_SCOPE_AGENT); }
DI float half_sum(float v) {
    const unsigned u = __float_as_uint(v);
    const auto r = __builtin_amdgcn_permlane32_swap(u, u, false, false);
    return __uint_as_float(r[0]) + __uint_as_float(r[1]);
}
#define BAR() asm volatile("s_barrier" ::: "memory")
#define LGKM_BAR() asm volatile("s_waitcnt lgkmcnt(0)\n\ts_barrier" ::: "memory")
#define VM_BARN(n) asm volatile("s_waitcnt vmcnt(%0)\n\ts_barrier" :: "n"(n) : "memory")
#define VM_BAR(n) asm volatile("s_waitcnt vmcnt(" #n ")\n\ts_barrier" ::: "memory")

#define XPOL ""
#define WPOL ""
DI void dmax(unsigned ldsaddr, unsigned voff, __amdgpu_buffer_rsrc_t srd, unsigned soff) {
    asm volatile("s_mov_b32 m0, %0\n\ts_nop 0\n\tbuffer_load_dwordx4 %1, %2, %3 offen " XPOL " lds"
                 :: "s"(ldsaddr), "v"(voff), "s"(srd), "s"(soff) : "m0", "memory");
}
DI void dmaw(unsigned ldsaddr, unsigned voff, __amdgpu_buffer_rsrc_t srd, unsigned soff) {
    asm volatile("s_mov_b32 m0, %0\n\ts_nop 0\n\tbuffer_load_dwordx4 %1, %2, %3 offen " WPOL " lds"
                 :: "s"(ldsaddr), "v"(voff), "s"(srd), "s"(soff) : "m0", "memory");
}

DI float quad_sum(float v) {
    const unsigned u = __float_as_uint(v);
    const auto r = __builtin_amdgcn_permlane16_swap(u, u, false, false);
    return half_sum(__uint_as_float(r[0]) + __uint_as_float(r[1]));
}
DI bf16x8 ld_a(const LAS unsigned char* p) { return *(const LAS bf16x8*)p; }
DI bf16x8 ld_b(const LAS unsigned char* p) {
    const s16x4 lo = __builtin_amdgcn_ds_read_tr16_b64_v4i16((LAS s16x4*)p);
    const s16x4 hi = __builtin_amdgcn_ds_read_tr16_b64_v4i16((LAS s16x4*)(p + 512));
    const s16x8 r = __builtin_shufflevector(lo, hi, 0, 1, 2, 3, 4, 5, 6, 7);
    return __builtin_bit_cast(bf16x8, r);
}

struct Op { int a, s, u; };
constexpr Op op_of(int nt, int m) {
    int k = 0;
    for (int a = 0; a < 7 + nt; ++a)
        for (int s = 0; s < nt; ++s)
            if (a - s >= 0 && a - s < 8)
                for (int u = 1; u >= 0; --u) { if (k == m) return Op{a, s, u}; ++k; }
    return Op{-1, -1, -1};
}
#define BARL 27
constexpr int BAR_E = 36, BAR_O = 18, BAR_L = BARL;
constexpr int rd_even(int m) {
    switch (m) {
        case 2: return 4; case 7: return 5; case 12: return 6; case 17: return 7; case 21: return 8; case 25: return 9;
        case 36: return 21; case 37: return 20; case 38: return 10; case 39: return 11; case 40: return 23; case 41: return 22;
        case 42: return 12; case 43: return 13; case 45: return 14;
        default: return -1;
    }
}
constexpr int rd_odd(int m) {
    switch (m) {
        case 0: return 5; case 2: return 6; case 4: return 7; case 6: return 8;
        case 18: return 21; case 19: return 20; case 20: return 10; case 21: return 11; case 22: return 23; case 23: return 22;
        case 24: return 12; case 25: return 25; case 26: return 24; case 28: return 13;
        default: return -1;
    }
}
struct Ptr { unsigned xa, xb, we[2], wo[2]; };
DI const LAS unsigned char* lds_at(unsigned a) { return (const LAS unsigned char*)(size_t)a; }
template <bool ODD> DI bf16x8 rd_x(const Ptr& p, int a) {
    return a < 8 ? ld_a(lds_at(p.xa) + (ODD ? 16384 : 0) + a * 2048) : ld_a(lds_at(p.xb) + (ODD ? 16384 : 0) + (a - 8) * 2048);
}
template <bool ODD> DI bf16x8 rd_w(const Ptr& p, int s, int u) { return ld_b(lds_at(ODD ? p.wo[u] : p.we[u]) + s * 8192); }
DI void mma(f32x4& c, const bf16x8& a, const bf16x8& b) {
    asm volatile("v_mfma_f32_16x16x32_bf16 %0, %1, %2, %0" : "+v"(c) : "v"(a), "v"(b));
}
DI void flip(unsigned& a, int d) { asm volatile("v_add_u32 %0, %1, %0" : "+v"(a) : "s"(d)); }
DI void negate(int& d) { asm volatile("s_sub_i32 %0, 0, %0" : "+s"(d) :: "scc"); }
template <bool ODD, int M>
DI void stage_ops(f32x4 (&acc)[8][2], bf16x8 (&xa)[10], const bf16x8 (&wc)[3][2], bf16x8 (&wn)[3][2], Ptr& p, int& dx, int& de, int& dq) {
    constexpr int NT = ODD ? 2 : 3, NM = 16 * NT;
    if constexpr (M < NM) {
        if constexpr (M == (ODD ? BAR_O : BAR_E)) { LGKM_BAR(); __builtin_amdgcn_sched_barrier(0); }
        constexpr Op o = op_of(NT, M);
        mma(acc[o.a - o.s][o.u], xa[o.a], wc[o.s][o.u]);
        __builtin_amdgcn_sched_barrier(0);
        constexpr int rd = ODD ? rd_odd(M) : rd_even(M);
        if constexpr (rd >= 0 && rd < 10) xa[rd] = rd_x<ODD>(p, rd);
        if constexpr (rd >= 10 && rd < 20) xa[rd - 10] = rd_x<!ODD>(p, rd - 10);
        if constexpr (rd >= 20) wn[(rd - 20) >> 1][rd & 1] = rd_w<!ODD>(p, (rd - 20) >> 1, rd & 1);
        if constexpr (ODD && M == 8) flip(p.xa, dx);
        if constexpr (ODD && M == 9) flip(p.xb, dx);
        if constexpr (ODD && M == 10) flip(p.we[0], de);
        if constexpr (ODD && M == 11) flip(p.we[1], de);
        if constexpr (ODD && M == 12) flip(p.wo[0], dq);
        if constexpr (ODD && M == 13) flip(p.wo[1], dq);
        if constexpr (ODD && M == 14) { negate(dx); negate(de); }
        if constexpr (ODD && M == 15) negate(dq);
        __builtin_amdgcn_sched_barrier(0);
        stage_ops<ODD, M + 1>(acc, xa, wc, wn, p, dx, de, dq);
    }
}
template <int M>
DI void stage_last(f32x4 (&acc)[8][2], bf16x8 (&xa)[10], const bf16x8 (&wc)[3][2], f32x4 (&rc)[8], const Ptr& p,
                   LAS unsigned char* snd, const LAS unsigned char* rcv) {
    if constexpr (M < 32) {
        if constexpr (M == BAR_L) { LGKM_BAR(); __builtin_amdgcn_sched_barrier(0); }
        constexpr Op o = op_of(2, 2 * (M & 15));
        constexpr int u = M < 16 ? 1 : 0;
        mma(acc[o.a - o.s][u], xa[o.a], wc[o.s][u]);
        __builtin_amdgcn_sched_barrier(0);
        if constexpr (M < 4) xa[5 + M] = rd_x<true>(p, 5 + M);
        if constexpr (M >= 10 && M <= 22 && (M & 1) == 0) *(LAS f32x4*)(snd + ((M - 10) >> 1) * 1024) = acc[(M - 10) >> 1][1];
        if constexpr (M == 23) *(LAS f32x4*)(snd + 7 * 1024) = acc[7][1];
        if constexpr (M >= BAR_L) {
            constexpr int n0 = (M - BAR_L) * 8 / (32 - BAR_L), n1 = (M + 1 - BAR_L) * 8 / (32 - BAR_L);
#pragma unroll
            for (int j = n0; j < n1; ++j) rc[j] = *(const LAS f32x4*)(rcv + j * 1024);
        }
        __builtin_amdgcn_sched_barrier(0);
        stage_last<M + 1>(acc, xa, wc, rc, p, snd, rcv);
    }
}

__global__ void __launch_bounds__(512, 2)
conv_stem_fused(const u16* __restrict__ x, const u16* __restrict__ w, const u16* __restrict__ bias,
                const u16* __restrict__ gamma, const u16* __restrict__ beta, u16* __restrict__ out,
                unsigned char* __restrict__ ws) {
    __shared__ __attribute__((aligned(16))) unsigned char smem[LDS_BYTES];
    const unsigned tpre = (unsigned)__builtin_amdgcn_s_memrealtime();
    __builtin_amdgcn_sched_barrier(0);
    asm volatile("" :: "s"(ws));
    __builtin_amdgcn_sched_barrier(0);
    const unsigned tka = (unsigned)__builtin_amdgcn_s_memrealtime();
    __builtin_amdgcn_sched_barrier(0);
    unsigned tb0 = 0, tl1 = 0;
    LAS unsigned char* L = (LAS unsigned char*)smem;
    const unsigned lbase = (unsigned)(size_t)L;

    const int tid = threadIdx.x, lane = tid & 63;
    const int wave = __builtin_amdgcn_readfirstlane(tid >> 6);
    const int bid = blockIdx.x, xcd = bid & 7, idx = bid >> 3;
    const int R = 8 * (xcd >> 1) + (idx & 7), ct = 4 * (xcd & 1) + (idx >> 3);
    const int b = R >> 4, rt = R & 15, grp = b * 8 + ct;
    const int l0 = rt * TS, nv = rt == 15 ? L_ - 15 * TS : TS;
    u64* const box = (u64*)(ws + WS_BOX) + grp * 1024;
    u64* const flags = (u64*)(ws + WS_FLAG) + grp * 16;
    u64* const epoch = (u64*)(ws + WS_EPOCH + grp * 256);
    u64* const hint = (u64*)(ws + WS_HINT) + grp * 256;
    STAMP(0);

    const int c16 = lane & 15, g4 = lane >> 4;
    const int r8 = lane >> 3, c8 = lane & 7;
    f32x4 v[2][2];
    const LAS unsigned char* const yr = L + YB + (16 * wave + r8) * 128 + ((c8 ^ (wave & 2)) << 4);

    if (wave >= 4) {
        const int lw = wave - 4;
        const __amdgpu_buffer_rsrc_t sx = __builtin_amdgcn_make_buffer_rsrc((void*)x, 0, 2 * T_ * D_ * 2, 0x00020000);
        const __amdgpu_buffer_rsrc_t sw = __builtin_amdgcn_make_buffer_rsrc((void*)w, 0, 5 * D_ * D_ * 2, 0x00020000);
        const u16 gam16 = gamma[ct * 64 + 16 * lw + c16], bet16 = beta[ct * 64 + 16 * lw + c16];
        unsigned vx[4];
#pragma unroll
        for (int n = 0; n < 4; ++n) {
            const int p = min(l0 + 64 * (lw & 1) + 8 * r8 + 2 * n + (lw >> 1), T_ / 2 - 1);
            vx[n] = p * 2048 + ((c8 ^ (r8 & 6)) << 4);
        }
        const unsigned vw = r8 * 1024 + ((c8 ^ (r8 & 2) ^ ((lw & 1) << 2)) << 4);
        const unsigned sx0 = b * (T_ * D_ * 2), sw0 = ct * 128 + lw * 8192;
        const unsigned mx0 = lbase + XS + lw * 1024, mwe0 = lbase + WE + lw * 1024, mwo0 = lbase + WO + lw * 1024;
        const int rot = ROT_EXPR;
        auto issue_e = [&](int cc, int lo, int hi) {
            const int cr = (cc + rot) & 7;
            const unsigned so = sx0 + cr * 128, mx = mx0 + (cc & 1) * 32768, mw = mwe0 + (cc & 1) * 24576, wo = sw0 + cr * 65536;
#pragma unroll
            for (int n = 0; n < 4; ++n) if (n >= lo && n < hi) dmax(mx + n * 4096, vx[n], sx, so);
#pragma unroll
            for (int ti = 0; ti < 3; ++ti)
#pragma unroll
                for (int h = 0; h < 2; ++h) if (4 + 2 * ti + h >= lo && 4 + 2 * ti + h < hi) dmaw(mw + ti * 8192 + h * 4096, vw, sw, wo + ti * 2 * 524288 + h * 32768);
        };
        auto issue_o = [&](int cc) {
            const int cr = (cc + rot) & 7;
            const unsigned so = sx0 + cr * 128 + 1024, mx = mx0 + (cc & 1) * 32768 + 16384, mw = mwo0 + (cc & 1) * 16384, wo = sw0 + cr * 65536 + 524288;
#pragma unroll
            for (int n = 0; n < 4; ++n) dmax(mx + n * 4096, vx[n], sx, so);
#pragma unroll
            for (int ti = 0; ti < 2; ++ti)
#pragma unroll
                for (int h = 0; h < 2; ++h) dmaw(mw + ti * 8192 + h * 4096, vw, sw, wo + ti * 2 * 524288 + h * 32768);
        };
        issue_e(0, 0, 10); issue_o(0);
        STAMP(1);
        VM_BAR(8);
        STAMP(2);
        issue_e(1, 0, 10); VM_BAR(10);
        issue_o(1); VM_BAR(8);
        issue_e(2, 0, 10); issue_o(2); VM_BAR(18);
        issue_e(3, 0, SPLIT); VM_BARN(8 + SPLIT);
        for (int cc = 2; cc < 6; ++cc) {
            issue_e(cc + 1, SPLIT, 10); issue_o(cc + 1); VM_BAR(18);
            issue_e(cc + 2, 0, SPLIT); VM_BARN(8 + SPLIT);
        }
        issue_e(7, SPLIT, 10); issue_o(7); VM_BAR(18);
        VM_BAR(8);
        VM_BAR(0);
        STAMP(3);
        const u64 e1 = *(volatile LAS u64*)(L + L_EP) + 1;
        u64 fl = lane < 16 ? ld_dev(flags + lane) : e1;
        const unsigned th = (unsigned)__builtin_amdgcn_s_memrealtime();
        if (lw == 0 && lane == 0) st_dev(hint + rt * 16, (e1 << 32) | th);
        BAR();
        STAMP(4);
        for (unsigned sp = 0; __any(fl != e1) && sp < SPIN_MAX; ++sp) fl = lane < 16 ? ld_dev(flags + lane) : e1;
        if (rt == 0 && lw == 0 && lane == 0) st_dev(epoch, e1);
        volatile LAS unsigned* tpw = (volatile LAS unsigned*)(L + L_TP);
        if (lw == 0) {
            u64 h = (e1 << 32) | th;
            for (unsigned sp = 0; sp < SPIN_MAX; ++sp) {
                if (lane < 16) h = ld_dev(hint + lane * 16);
                if (!__any((unsigned)(h >> 32) != (unsigned)e1)) break;
            }
            int d = (unsigned)(h >> 32) == (unsigned)e1 ? (int)((unsigned)h - th) : 0;
#pragma unroll
            for (int o = 1; o < 16; o <<= 1) d = max(d, __shfl_xor(d, o));
            d = min(max(d, 0), 1000);
            if (lane == 0) tpw[4] = 0x80000000u | (unsigned)d;
        }
        unsigned tp = 0, dl = 0;
        for (unsigned sp = 0; sp < SPIN_MAX * 16; ++sp) {
            tp = tpw[lw]; dl = tpw[4];
            if (tp != 0 && dl != 0) break;
            __builtin_amdgcn_s_sleep(1);
        }
        const unsigned tstart = tp + (dl & 0xFFFFu) + MARGIN;
        while ((int)((unsigned)__builtin_amdgcn_s_memrealtime() - tstart) < 0) {}
        const u64* src = box + g4 * 256 + 16 * lw + c16;
        u64 g[4];
        STAMP(8);
#pragma unroll
        for (int i = 0; i < 4; ++i) g[i] = ld_dev(src + i * 64);
        BAR();
#pragma unroll
        for (int u = 0; u < 2; ++u)
#pragma unroll
            for (int p = 0; p < 2; ++p) v[u][p] = *(const LAS f32x4*)(yr + p * YPL + u * 1024);
        for (unsigned sp = 0; sp < SPIN_MAX; ++sp) {
            bool bad = false;
#pragma unroll
            for (int i = 0; i < 4; ++i) bad |= (unsigned)(g[i] >> 32) == 0xFFFFFFFFu;
            const bool more = __any(bad);
            if (!more) break;
#pragma unroll
            for (int i = 0; i < 4; ++i) g[i] = ld_dev(src + i * 64);
        }
        STAMP(5);
        float n[4], s = 0.f;
#pragma unroll
        for (int i = 0; i < 4; ++i) {
            n[i] = (float)TS - ((i == 3 && g4 == 3) ? (float)(16 * TS - L_) : 0.f);
            s += n[i] * __uint_as_float((unsigned)g[i]);
        }
        const float mean = quad_sum(s) * (1.f / L_);
        float q = 0.f;
#pragma unroll
        for (int i = 0; i < 4; ++i) { const float d = __uint_as_float((unsigned)g[i]) - mean; q += __uint_as_float((unsigned)(g[i] >> 32)) + n[i] * d * d; }
        const float sc = bf2f(gam16) * __builtin_amdgcn_rsqf(quad_sum(q) * (1.f / L_) + 1e-5f);
        if (lane < 16) {
            *(LAS float*)(L + L_AC + (16 * lw + lane) * 4) = sc;
            *(LAS float*)(L + L_AC + 256 + (16 * lw + lane) * 4) = bf2f(bet16) - mean * sc;
        }
    } else {
        const int kh = wave & 1, chh = wave >> 1;
        const float b0 = bf2f(bias[ct * 64 + 16 * wave + c16]);
        f32x4 acc[8][2];
#pragma unroll
        for (int j = 0; j < 8; ++j) {
            acc[j][0] = f32x4{b0, b0, b0, b0}; acc[j][1] = f32x4{0.f, 0.f, 0.f, 0.f};
            asm volatile("" : "+v"(acc[j][0]), "+v"(acc[j][1]));
        }
        u64 e0 = 0;
        if (wave == 0) {
            e0 = ld_dev(epoch);
            st_dev(box + rt * 64 + lane, BOX_EMPTY);
            if (lane < 5) *(volatile LAS unsigned*)(L + L_TP + lane * 4) = 0;
        }
        Ptr p;
        p.xa = lbase + XS + c16 * 128 + (((4 * kh + g4) ^ (c16 & 6)) << 4);
        p.xb = lbase + XS + (c16 + 1) * 128 + (((4 * kh + g4) ^ ((c16 + 1) & 6)) << 4);
        {
            const int q4 = c16 >> 2, p4 = lane & 3;
#pragma unroll
            for (int u = 0; u < 2; ++u) {
                const unsigned wl = kh * 4096 + (8 * g4 + q4) * 128 + (((4 * chh + 2 * (u ^ kh) + (p4 >> 1)) ^ (q4 & 2) ^ ((g4 & 1) << 2)) << 4) + 8 * (p4 & 1);
                p.we[u] = lbase + WE + wl; p.wo[u] = lbase + WO + wl;
            }
        }
        asm volatile("" : "+v"(p.xa), "+v"(p.xb), "+v"(p.we[0]), "+v"(p.we[1]), "+v"(p.wo[0]), "+v"(p.wo[1]));
        LAS unsigned char* const snd = L + EX + wave * 8192 + lane * 16;
        bf16x8 xa[10], we[3][2], wo[3][2];
        BAR();
        tb0 = (unsigned)__builtin_amdgcn_s_memrealtime();
        __builtin_amdgcn_sched_barrier(0);
#define SB() __builtin_amdgcn_sched_barrier(0)
        we[0][1] = rd_w<false>(p, 0, 1); SB(); we[0][0] = rd_w<false>(p, 0, 0); SB();
        xa[0] = rd_x<false>(p, 0); SB(); xa[1] = rd_x<false>(p, 1); SB();
        we[1][1] = rd_w<false>(p, 1, 1); SB(); we[1][0] = rd_w<false>(p, 1, 0); SB();
        xa[2] = rd_x<false>(p, 2); SB();
        we[2][1] = rd_w<false>(p, 2, 1); SB(); we[2][0] = rd_w<false>(p, 2, 0); SB();
        xa[3] = rd_x<false>(p, 3); SB();
#undef SB
        f32x4 y[8];
        const LAS unsigned char* const rcv = L + EX + (wave ^ 1) * 8192 + lane * 16;
        int dx = 32768, de = 24576, dq = 16384;
        const int trig = wave == 0 ? 1 : -1;
#pragma unroll 1
        for (int it = 0; it < 7; ++it) {
            stage_ops<false, 0>(acc, xa, we, wo, p, dx, de, dq);
            stage_ops<true, 0>(acc, xa, wo, we, p, dx, de, dq);
            int itv = it;
            asm volatile("" : "+s"(itv));
            if (itv == trig) {
                asm volatile("s_waitcnt vmcnt(0)" : "+v"(e0) :: "memory");
                if (lane == 0) { *(volatile LAS u64*)(L + L_EP) = e0; st_dev(flags + rt, e0 + 1); }
            }
        }
        stage_ops<false, 0>(acc, xa, we, wo, p, dx, de, dq);
        stage_last<0>(acc, xa, wo, y, p, snd, rcv);
        asm volatile("s_nop 7" : "+v"(acc[0][0]), "+v"(acc[1][0]), "+v"(acc[2][0]), "+v"(acc[3][0]),
                                 "+v"(acc[4][0]), "+v"(acc[5][0]), "+v"(acc[6][0]), "+v"(acc[7][0]));
        tl1 = (unsigned)__builtin_amdgcn_s_memrealtime();
        __builtin_amdgcn_sched_barrier(0);
#pragma unroll
        for (int j = 0; j < 8; ++j) y[j] += acc[j][0];
        STAMP(3);
        {
            const bool k5 = g4 == 3, k3 = k5 && rt == 15;
            f32x2 s0 = {0.f, 0.f}, s1 = {0.f, 0.f};
#pragma unroll
            for (int j = 0; j < 8; ++j) {
                const float y3 = (j >= 5 ? k5 : j >= 3 ? k3 : false) ? 0.f : y[j][3];
                s0 += f32x2{y[j][0], y[j][1]}; s1 += f32x2{y[j][2], y3};
            }
            s0 += s1;
            const float m = quad_sum(s0[0] + s0[1]) * (rt == 15 ? 1.f / (L_ - 15 * TS) : 1.f / TS);
            const f32x2 mm = {m, m};
            f32x2 q0 = {0.f, 0.f}, q1 = {0.f, 0.f};
#pragma unroll
            for (int j = 0; j < 8; ++j) {
                const float y3 = (j >= 5 ? k5 : j >= 3 ? k3 : false) ? m : y[j][3];
                const f32x2 d0 = f32x2{y[j][0], y[j][1]} - mm, d1 = f32x2{y[j][2], y3} - mm;
                q0 += d0 * d0; q1 += d1 * d1;
            }
            q0 += q1;
            const float m2 = quad_sum(q0[0] + q0[1]);
            unsigned m2u = __float_as_uint(m2);
            if (m2u == 0xFFFFFFFFu) m2u = 0x7FFFFFFFu;
            if (lane < 16) st_dev(box + rt * 64 + 16 * wave + lane, ((u64)m2u << 32) | __float_as_uint(m));
            const unsigned tp = (unsigned)__builtin_amdgcn_s_memrealtime() | 1u;
            if (lane == 0) *(volatile LAS unsigned*)(L + L_TP + wave * 4) = tp;
        }
        STAMP(4);
        {
            const int word = (8 * wave + 4 * (c16 >> 3) + (c16 & 3)) ^ ((g4 & 1) << 3);
            LAS unsigned char* yp = L + YB + ((c16 >> 2) & 1) * YPL + 32 * g4 * 128 + word * 4;
#pragma unroll
            for (int j = 0; j < 8; ++j)
#pragma unroll
                for (int i = 0; i < 4; ++i) *(LAS float*)(yp + (8 * i + j) * 128) = y[j][i];
        }
        LGKM_BAR();
#pragma unroll
        for (int u = 0; u < 2; ++u)
#pragma unroll
            for (int pl = 0; pl < 2; ++pl) v[u][pl] = *(const LAS f32x4*)(yr + pl * YPL + u * 1024);
    }
    LGKM_BAR();
    STAMP(6);
    f32x4 sa[2], sc[2];
#pragma unroll
    for (int p = 0; p < 2; ++p) {
        sa[p] = *(const LAS f32x4*)(L + L_AC + c8 * 32 + p * 16);
        sc[p] = *(const LAS f32x4*)(L + L_AC + 256 + c8 * 32 + p * 16);
    }
    constexpr float K1 = -2.0f * 1.4426950408889634f * 0.7978845608028654f, K2 = K1 * 0.044715f;
#pragma unroll
    for (int u = 0; u < 2; ++u) {
        const int t = 16 * wave + 8 * u + r8;
        unsigned o[4];
#pragma unroll
        for (int h = 0; h < 4; ++h) {
            const int p = h >> 1, e = (h & 1) * 2;
            const f32x2 z = f32x2{v[u][p][e], v[u][p][e + 1]} * f32x2{sa[p][e], sa[p][e + 1]} + f32x2{sc[p][e], sc[p][e + 1]};
            const f32x2 arg = z * (z * z * K2 + K1);
            const f32x2 dn = f32x2{__builtin_amdgcn_exp2f(arg[0]), __builtin_amdgcn_exp2f(arg[1])} + 1.f;
            const f32x2 r = z * f32x2{__builtin_amdgcn_rcpf(dn[0]), __builtin_amdgcn_rcpf(dn[1])};
            o[h] = __builtin_bit_cast(unsigned, bf16x2{(__bf16)r[0], (__bf16)r[1]});
        }
        if (t < nv) {
            const u32x4 ov = u32x4{o[0], o[1], o[2], o[3]};
            u16* op = out + ((size_t)(b * L_ + l0 + t) * D_ + ct * 64 + c8 * 8);
            asm volatile("global_store_dwordx4 %0, %1, off " STPOL :: "v"(op), "v"(ov) : "memory");
        }
    }
    if (wave == 0 && (bid == 101)) {
        const unsigned tend = (unsigned)__builtin_amdgcn_s_memrealtime();
        const unsigned d = (tka - tpre) * 100;
        while ((unsigned)__builtin_amdgcn_s_memrealtime() - tend < d) __builtin_amdgcn_s_sleep(2);
    }
}

extern "C" void kernel_launch(void* const* d_in, const int* in_sizes, int n_in, void* d_out, int out_size,
                              void* d_ws, size_t ws_size, hipStream_t stream) {
    conv_stem_fused<<<256, 512, 0, stream>>>((const u16*)d_in[0], (const u16*)d_in[1], (const u16*)d_in[2],
                                             (const u16*)d_in[3], (const u16*)d_in[4], (u16*)d_out, (unsigned char*)d_ws);
}
```

```cpp
#include <hip/hip_runtime.h>
#include <stdint.h>

typedef unsigned short u16;
typedef unsigned long long u64;
typedef __attribute__((ext_vector_type(8))) __bf16 bf16x8;
typedef __attribute__((ext_vector_type(2))) __bf16 bf16x2;
typedef __attribute__((ext_vector_type(4))) short s16x4;
typedef __attribute__((ext_vector_type(8))) short s16x8;
typedef __attribute__((ext_vector_type(16))) float f32x16;
typedef __attribute__((ext_vector_type(4))) float f32x4;
typedef __attribute__((ext_vector_type(2))) float f32x2;
typedef __attribute__((ext_vector_type(4))) unsigned u32x4;
#define LAS __attribute__((address_space(3)))
#define DI __device__ __forceinline__

constexpr int T_ = 4000, D_ = 512, L_ = 1998, TS = 125;

constexpr unsigned XS = 0;
constexpr unsigned WE = 65536;
constexpr unsigned WO = WE + 49152;
constexpr unsigned MISC = WO + 32768;
constexpr unsigned L_EP = MISC;
constexpr unsigned L_AC = MISC + 64;
constexpr unsigned L_TP = L_AC + 512;
constexpr unsigned LDS_BYTES = L_TP + 64;
constexpr unsigned EX = 0;
constexpr unsigned YB = 32768;
constexpr unsigned YPL = 16384 + 64;

constexpr size_t WS_EPOCH = 0;
constexpr size_t WS_FLAG = 4096;
constexpr size_t WS_HINT = 8192;
constexpr size_t WS_BOX = 65536;
#define STAMP(i) do {} while (0)

#define ROT_EXPR ((R + 2 * ct) & 7)
#define PER_A 2
#define PER_B 2
#define SPLIT 7
#define MARGIN 15
#define PRO 1
#define STPOL "sc1"
constexpr u64 BOX_EMPTY = 0xFFFFFFFFFFFFFFFFull;
constexpr unsigned SPIN_MAX = 1u << 16;

DI float bf2f(u16 v) { return __uint_as_float(((unsigned)v) << 16); }
DI u64 ld_dev(const u64* p) { return __hip_atomic_load(p, __ATOMIC_RELAXED, __HIP_MEMORY_SCOPE_AGENT); }
DI void st_dev(u64* p, u64 v) { __hip_atomic_store(p, v, __ATOMIC_RELAXED, __HIP_MEMORY_SCOPE_AGENT); }
DI float half_sum(float v) {
    const unsigned u = __float_as_uint(v);
    const auto r = __builtin_amdgcn_permlane32_swap(u, u, false, false);
    return __uint_as_float(r[0]) + __uint_as_float(r[1]);
}
#define BAR() asm volatile("s_barrier" ::: "memory")
#define LGKM_BAR() asm volatile("s_waitcnt lgkmcnt(0)\n\ts_barrier" ::: "memory")
#define VM_BARN(n) asm volatile("s_waitcnt vmcnt(%0)\n\ts_barrier" :: "n"(n) : "memory")
#define VM_BAR(n) asm volatile("s_waitcnt vmcnt(" #n ")\n\ts_barrier" ::: "memory")

#define XPOL ""
#define WPOL ""
DI void dmax(unsigned ldsaddr, unsigned voff, __amdgpu_buffer_rsrc_t srd, unsigned soff) {
    asm volatile("s_mov_b32 m0, %0\n\ts_nop 0\n\tbuffer_load_dwordx4 %1, %2, %3 offen " XPOL " lds"
                 :: "s"(ldsaddr), "v"(voff), "s"(srd), "s"(soff) : "m0", "memory");
}
DI void dmaw(unsigned ldsaddr, unsigned voff, __amdgpu_buffer_rsrc_t srd, unsigned soff) {
    asm volatile("s_mov_b32 m0, %0\n\ts_nop 0\n\tbuffer_load_dwordx4 %1, %2, %3 offen " WPOL " lds"
                 :: "s"(ldsaddr), "v"(voff), "s"(srd), "s"(soff) : "m0", "memory");
}

DI float quad_sum(float v) {
    const unsigned u = __float_as_uint(v);
    const auto r = __builtin_amdgcn_permlane16_swap(u, u, false, false);
    return half_sum(__uint_as_float(r[0]) + __uint_as_float(r[1]));
}
DI bf16x8 ld_a(const LAS unsigned char* p) { return *(const LAS bf16x8*)p; }
DI bf16x8 ld_b(const LAS unsigned char* p) {
    const s16x4 lo = __builtin_amdgcn_ds_read_tr16_b64_v4i16((LAS s16x4*)p);
    const s16x4 hi = __builtin_amdgcn_ds_read_tr16_b64_v4i16((LAS s16x4*)(p + 512));
    const s16x8 r = __builtin_shufflevector(lo, hi, 0, 1, 2, 3, 4, 5, 6, 7);
    return __builtin_bit_cast(bf16x8, r);
}

struct Op { int a, s, u; };
constexpr Op op_of(int nt, int m) {
    int k = 0;
    for (int a = 0; a < 7 + nt; ++a)
        for (int s = 0; s < nt; ++s)
            if (a - s >= 0 && a - s < 8)
                for (int u = 1; u >= 0; --u) { if (k == m) return Op{a, s, u}; ++k; }
    return Op{-1, -1, -1};
}
#define BARL 27
constexpr int BAR_E = 36, BAR_O = 18, BAR_L = BARL;
constexpr int rd_even(int m) {
    switch (m) {
        case 2: return 4; case 7: return 5; case 12: return 6; case 17: return 7; case 21: return 8; case 25: return 9;
        case 36: return 21; case 37: return 20; case 38: return 10; case 39: return 11; case 40: return 23; case 41: return 22;
        case 42: return 12; case 43: return 13; case 45: return 14;
        default: return -1;
    }
}
constexpr int rd_odd(int m) {
    switch (m) {
        case 0: return 5; case 2: return 6; case 4: return 7; case 6: return 8;
        case 18: return 21; case 19: return 20; case 20: return 10; case 21: return 11; case 22: return 23; case 23: return 22;
        case 24: return 12; case 25: return 25; case 26: return 24; case 28: return 13;
        default: return -1;
    }
}
struct Ptr { unsigned xa, xb, we[2], wo[2]; };
DI const LAS unsigned char* lds_at(unsigned a) { return (const LAS unsigned char*)(size_t)a; }
template <bool ODD> DI bf16x8 rd_x(const Ptr& p, int a) {
    return a < 8 ? ld_a(lds_at(p.xa) + (ODD ? 16384 : 0) + a * 2048) : ld_a(lds_at(p.xb) + (ODD ? 16384 : 0) + (a - 8) * 2048);
}
template <bool ODD> DI bf16x8 rd_w(const Ptr& p, int s, int u) { return ld_b(lds_at(ODD ? p.wo[u] : p.we[u]) + s * 8192); }
DI void mma(f32x4& c, const bf16x8& a, const bf16x8& b) {
    asm volatile("v_mfma_f32_16x16x32_bf16 %0, %1, %2, %0" : "+v"(c) : "v"(a), "v"(b));
}
DI void flip(unsigned& a, int d) { asm volatile("v_add_u32 %0, %1, %0" : "+v"(a) : "s"(d)); }
DI void negate(int& d) { asm volatile("s_sub_i32 %0, 0, %0" : "+s"(d) :: "scc"); }
template <bool ODD, int M>
DI void stage_ops(f32x4 (&acc)[8][2], bf16x8 (&xa)[10], const bf16x8 (&wc)[3][2], bf16x8 (&wn)[3][2], Ptr& p, int& dx, int& de, int& dq) {
    constexpr int NT = ODD ? 2 : 3, NM = 16 * NT;
    if constexpr (M < NM) {
        if constexpr (M == (ODD ? BAR_O : BAR_E)) { LGKM_BAR(); __builtin_amdgcn_sched_barrier(0); }
        constexpr Op o = op_of(NT, M);
        mma(acc[o.a - o.s][o.u], xa[o.a], wc[o.s][o.u]);
        __builtin_amdgcn_sched_barrier(0);
        constexpr int rd = ODD ? rd_odd(M) : rd_even(M);
        if constexpr (rd >= 0 && rd < 10) xa[rd] = rd_x<ODD>(p, rd);
        if constexpr (rd >= 10 && rd < 20) xa[rd - 10] = rd_x<!ODD>(p, rd - 10);
        if constexpr (rd >= 20) wn[(rd - 20) >> 1][rd & 1] = rd_w<!ODD>(p, (rd - 20) >> 1, rd & 1);
        if constexpr (ODD && M == 8) flip(p.xa, dx);
        if constexpr (ODD && M == 9) flip(p.xb, dx);
        if constexpr (ODD && M == 10) flip(p.we[0], de);
        if constexpr (ODD && M == 11) flip(p.we[1], de);
        if constexpr (ODD && M == 12) flip(p.wo[0], dq);
        if constexpr (ODD && M == 13) flip(p.wo[1], dq);
        if constexpr (ODD && M == 14) { negate(dx); negate(de); }
        if constexpr (ODD && M == 15) negate(dq);
        __builtin_amdgcn_sched_barrier(0);
        stage_ops<ODD, M + 1>(acc, xa, wc, wn, p, dx, de, dq);
    }
}
template <int M>
DI void stage_last(f32x4 (&acc)[8][2], bf16x8 (&xa)[10], const bf16x8 (&wc)[3][2], f32x4 (&rc)[8], const Ptr& p,
                   LAS unsigned char* snd, const LAS unsigned char* rcv) {
    if constexpr (M < 32) {
        if constexpr (M == BAR_L) { LGKM_BAR(); __builtin_amdgcn_sched_barrier(0); }
        constexpr Op o = op_of(2, 2 * (M & 15));
        constexpr int u = M < 16 ? 1 : 0;
        mma(acc[o.a - o.s][u], xa[o.a], wc[o.s][u]);
        __builtin_amdgcn_sched_barrier(0);
        if constexpr (M < 4) xa[5 + M] = rd_x<true>(p, 5 + M);
        if constexpr (M >= 10 && M <= 22 && (M & 1) == 0) *(LAS f32x4*)(snd + ((M - 10) >> 1) * 1024) = acc[(M - 10) >> 1][1];
        if constexpr (M == 23) *(LAS f32x4*)(snd + 7 * 1024) = acc[7][1];
        if constexpr (M >= BAR_L) {
            constexpr int n0 = (M - BAR_L) * 8 / (32 - BAR_L), n1 = (M + 1 - BAR_L) * 8 / (32 - BAR_L);
#pragma unroll
            for (int j = n0; j < n1; ++j) rc[j] = *(const LAS f32x4*)(rcv + j * 1024);
        }
        __builtin_amdgcn_sched_barrier(0);
        stage_last<M + 1>(acc, xa, wc, rc, p, snd, rcv);
    }
}

__global__ void __launch_bounds__(512, 2)
conv_stem_fused(const u16* __restrict__ x, const u16* __restrict__ w, const u16* __restrict__ bias,
                const u16* __restrict__ gamma, const u16* __restrict__ beta, u16* __restrict__ out,
                unsigned char* __restrict__ ws) {
    __shared__ __attribute__((aligned(16))) unsigned char smem[LDS_BYTES];
    const unsigned tpre = (unsigned)__builtin_amdgcn_s_memrealtime();
    __builtin_amdgcn_sched_barrier(0);
    unsigned tA = tpre;
    LAS unsigned char* L = (LAS unsigned char*)smem;
    const unsigned lbase = (unsigned)(size_t)L;

    const int tid = threadIdx.x, lane = tid & 63;
    const int wave = __builtin_amdgcn_readfirstlane(tid >> 6);
    const int bid = blockIdx.x, xcd = bid & 7, idx = bid >> 3;
    const int R = 8 * (xcd >> 1) + (idx & 7), ct = 4 * (xcd & 1) + (idx >> 3);
    const int b = R >> 4, rt = R & 15, grp = b * 8 + ct;
    const int l0 = rt * TS, nv = rt == 15 ? L_ - 15 * TS : TS;
    u64* const box = (u64*)(ws + WS_BOX) + grp * 1024;
    u64* const flags = (u64*)(ws + WS_FLAG) + grp * 16;
    u64* const epoch = (u64*)(ws + WS_EPOCH + grp * 256);
    u64* const hint = (u64*)(ws + WS_HINT) + grp * 256;
    STAMP(0);

    const int c16 = lane & 15, g4 = lane >> 4;
    const int r8 = lane >> 3, c8 = lane & 7;
    f32x4 v[2][2];
    const LAS unsigned char* const yr = L + YB + (16 * wave + r8) * 128 + ((c8 ^ (wave & 2)) << 4);

    if (wave >= 4) {
        const int lw = wave - 4;
        const __amdgpu_buffer_rsrc_t sx = __builtin_amdgcn_make_buffer_rsrc((void*)x, 0, 2 * T_ * D_ * 2, 0x00020000);
        const __amdgpu_buffer_rsrc_t sw = __builtin_amdgcn_make_buffer_rsrc((void*)w, 0, 5 * D_ * D_ * 2, 0x00020000);
        const u16 gam16 = gamma[ct * 64 + 16 * lw + c16], bet16 = beta[ct * 64 + 16 * lw + c16];
        unsigned vx[4];
#pragma unroll
        for (int n = 0; n < 4; ++n) {
            const int p = min(l0 + 64 * (lw & 1) + 8 * r8 + 2 * n + (lw >> 1), T_ / 2 - 1);
            vx[n] = p * 2048 + ((c8 ^ (r8 & 6)) << 4);
        }
        const unsigned vw = r8 * 1024 + ((c8 ^ (r8 & 2) ^ ((lw & 1) << 2)) << 4);
        const unsigned sx0 = b * (T_ * D_ * 2), sw0 = ct * 128 + lw * 8192;
        const unsigned mx0 = lbase + XS + lw * 1024, mwe0 = lbase + WE + lw * 1024, mwo0 = lbase + WO + lw * 1024;
        const int rot = ROT_EXPR;
        auto issue_e = [&](int cc, int lo, int hi) {
            const int cr = (cc + rot) & 7;
            const unsigned so = sx0 + cr * 128, mx = mx0 + (cc & 1) * 32768, mw = mwe0 + (cc & 1) * 24576, wo = sw0 + cr * 65536;
#pragma unroll
            for (int n = 0; n < 4; ++n) if (n >= lo && n < hi) dmax(mx + n * 4096, vx[n], sx, so);
#pragma unroll
            for (int ti = 0; ti < 3; ++ti)
#pragma unroll
                for (int h = 0; h < 2; ++h) if (4 + 2 * ti + h >= lo && 4 + 2 * ti + h < hi) dmaw(mw + ti * 8192 + h * 4096, vw, sw, wo + ti * 2 * 524288 + h * 32768);
        };
        auto issue_o = [&](int cc) {
            const int cr = (cc + rot) & 7;
            const unsigned so = sx0 + cr * 128 + 1024, mx = mx0 + (cc & 1) * 32768 + 16384, mw = mwo0 + (cc & 1) * 16384, wo = sw0 + cr * 65536 + 524288;
#pragma unroll
            for (int n = 0; n < 4; ++n) dmax(mx + n * 4096, vx[n], sx, so);
#pragma unroll
            for (int ti = 0; ti < 2; ++ti)
#pragma unroll
                for (int h = 0; h < 2; ++h) dmaw(mw + ti * 8192 + h * 4096, vw, sw, wo + ti * 2 * 524288 + h * 32768);
        };
{ __builtin_amdgcn_sched_barrier(0);  tA = (unsigned)__builtin_amdgcn_s_memrealtime(); asm volatile("s_waitcnt lgkmcnt(0)" : "+s"(tA)); __builtin_amdgcn_sched_barrier(0); }
        issue_e(0, 0, 10); issue_o(0);
        STAMP(1);
        VM_BAR(8);
        STAMP(2);
        issue_e(1, 0, 10); VM_BAR(10);
        issue_o(1); VM_BAR(8);
        issue_e(2, 0, 10); issue_o(2); VM_BAR(18);
        issue_e(3, 0, SPLIT); VM_BARN(8 + SPLIT);
        for (int cc = 2; cc < 6; ++cc) {
            issue_e(cc + 1, SPLIT, 10); issue_o(cc + 1); VM_BAR(18);
            issue_e(cc + 2, 0, SPLIT); VM_BARN(8 + SPLIT);
        }
        issue_e(7, SPLIT, 10); issue_o(7); VM_BAR(18);
        VM_BAR(8);
        VM_BAR(0);
        STAMP(3);
        const u64 e1 = *(volatile LAS u64*)(L + L_EP) + 1;
        u64 fl = lane < 16 ? ld_dev(flags + lane) : e1;
        const unsigned th = (unsigned)__builtin_amdgcn_s_memrealtime();
        if (lw == 0 && lane == 0) st_dev(hint + rt * 16, (e1 << 32) | th);
        BAR();
        STAMP(4);
        for (unsigned sp = 0; __any(fl != e1) && sp < SPIN_MAX; ++sp) fl = lane < 16 ? ld_dev(flags + lane) : e1;
        if (rt == 0 && lw == 0 && lane == 0) st_dev(epoch, e1);
        volatile LAS unsigned* tpw = (volatile LAS unsigned*)(L + L_TP);
        if (lw == 0) {
            u64 h = (e1 << 32) | th;
            for (unsigned sp = 0; sp < SPIN_MAX; ++sp) {
                if (lane < 16) h = ld_dev(hint + lane * 16);
                if (!__any((unsigned)(h >> 32) != (unsigned)e1)) break;
            }
            int d = (unsigned)(h >> 32) == (unsigned)e1 ? (int)((unsigned)h - th) : 0;
#pragma unroll
            for (int o = 1; o < 16; o <<= 1) d = max(d, __shfl_xor(d, o));
            d = min(max(d, 0), 1000);
            if (lane == 0) tpw[4] = 0x80000000u | (unsigned)d;
        }
        unsigned tp = 0, dl = 0;
        for (unsigned sp = 0; sp < SPIN_MAX * 16; ++sp) {
            tp = tpw[lw]; dl = tpw[4];
            if (tp != 0 && dl != 0) break;
            __builtin_amdgcn_s_sleep(1);
        }
        const unsigned tstart = tp + (dl & 0xFFFFu) + MARGIN;
        while ((int)((unsigned)__builtin_amdgcn_s_memrealtime() - tstart) < 0) {}
        const u64* src = box + g4 * 256 + 16 * lw + c16;
        u64 g[4];
        STAMP(8);
#pragma unroll
        for (int i = 0; i < 4; ++i) g[i] = ld_dev(src + i * 64);
        BAR();
#pragma unroll
        for (int u = 0; u < 2; ++u)
#pragma unroll
            for (int p = 0; p < 2; ++p) v[u][p] = *(const LAS f32x4*)(yr + p * YPL + u * 1024);
        for (unsigned sp = 0; sp < SPIN_MAX; ++sp) {
            bool bad = false;
#pragma unroll
            for (int i = 0; i < 4; ++i) bad |= (unsigned)(g[i] >> 32) == 0xFFFFFFFFu;
            const bool more = __any(bad);
            if (!more) break;
#pragma unroll
            for (int i = 0; i < 4; ++i) g[i] = ld_dev(src + i * 64);
        }
        STAMP(5);
        float n[4], s = 0.f;
#pragma unroll
        for (int i = 0; i < 4; ++i) {
            n[i] = (float)TS - ((i == 3 && g4 == 3) ? (float)(16 * TS - L_) : 0.f);
            s += n[i] * __uint_as_float((unsigned)g[i]);
        }
        const float mean = quad_sum(s) * (1.f / L_);
        float q = 0.f;
#pragma unroll
        for (int i = 0; i < 4; ++i) { const float d = __uint_as_float((unsigned)g[i]) - mean; q += __uint_as_float((unsigned)(g[i] >> 32)) + n[i] * d * d; }
        const float sc = bf2f(gam16) * __builtin_amdgcn_rsqf(quad_sum(q) * (1.f / L_) + 1e-5f);
        if (lane < 16) {
            *(LAS float*)(L + L_AC + (16 * lw + lane) * 4) = sc;
            *(LAS float*)(L + L_AC + 256 + (16 * lw + lane) * 4) = bf2f(bet16) - mean * sc;
        }
    } else {
        const int kh = wave & 1, chh = wave >> 1;
        const float b0 = bf2f(bias[ct * 64 + 16 * wave + c16]);
        f32x4 acc[8][2];
#pragma unroll
        for (int j = 0; j < 8; ++j) {
            acc[j][0] = f32x4{b0, b0, b0, b0}; acc[j][1] = f32x4{0.f, 0.f, 0.f, 0.f};
            asm volatile("" : "+v"(acc[j][0]), "+v"(acc[j][1]));
        }
        u64 e0 = 0;
        if (wave == 0) {
            e0 = ld_dev(epoch);
            st_dev(box + rt * 64 + lane, BOX_EMPTY);
            if (lane < 5) *(volatile LAS unsigned*)(L + L_TP + lane * 4) = 0;
        }
        Ptr p;
        p.xa = lbase + XS + c16 * 128 + (((4 * kh + g4) ^ (c16 & 6)) << 4);
        p.xb = lbase + XS + (c16 + 1) * 128 + (((4 * kh + g4) ^ ((c16 + 1) & 6)) << 4);
        {
            const int q4 = c16 >> 2, p4 = lane & 3;
#pragma unroll
            for (int u = 0; u < 2; ++u) {
                const unsigned wl = kh * 4096 + (8 * g4 + q4) * 128 + (((4 * chh + 2 * (u ^ kh) + (p4 >> 1)) ^ (q4 & 2) ^ ((g4 & 1) << 2)) << 4) + 8 * (p4 & 1);
                p.we[u] = lbase + WE + wl; p.wo[u] = lbase + WO + wl;
            }
        }
        asm volatile("" : "+v"(p.xa), "+v"(p.xb), "+v"(p.we[0]), "+v"(p.we[1]), "+v"(p.wo[0]), "+v"(p.wo[1]));
        LAS unsigned char* const snd = L + EX + wave * 8192 + lane * 16;
        bf16x8 xa[10], we[3][2], wo[3][2];
        BAR();
        STAMP(1);
#define SB() __builtin_amdgcn_sched_barrier(0)
        we[0][1] = rd_w<false>(p, 0, 1); SB(); we[0][0] = rd_w<false>(p, 0, 0); SB();
        xa[0] = rd_x<false>(p, 0); SB(); xa[1] = rd_x<false>(p, 1); SB();
        we[1][1] = rd_w<false>(p, 1, 1); SB(); we[1][0] = rd_w<false>(p, 1, 0); SB();
        xa[2] = rd_x<false>(p, 2); SB();
        we[2][1] = rd_w<false>(p, 2, 1); SB(); we[2][0] = rd_w<false>(p, 2, 0); SB();
        xa[3] = rd_x<false>(p, 3); SB();
#undef SB
        f32x4 y[8];
        const LAS unsigned char* const rcv = L + EX + (wave ^ 1) * 8192 + lane * 16;
        int dx = 32768, de = 24576, dq = 16384;
        const int trig = wave == 0 ? 1 : -1;
#pragma unroll 1
        for (int it = 0; it < 7; ++it) {
            stage_ops<false, 0>(acc, xa, we, wo, p, dx, de, dq);
            stage_ops<true, 0>(acc, xa, wo, we, p, dx, de, dq);
            int itv = it;
            asm volatile("" : "+s"(itv));
            if (itv == trig) {
                asm volatile("s_waitcnt vmcnt(0)" : "+v"(e0) :: "memory");
                if (lane == 0) { *(volatile LAS u64*)(L + L_EP) = e0; st_dev(flags + rt, e0 + 1); }
            }
        }
        stage_ops<false, 0>(acc, xa, we, wo, p, dx, de, dq);
        stage_last<0>(acc, xa, wo, y, p, snd, rcv);
        asm volatile("s_nop 7" : "+v"(acc[0][0]), "+v"(acc[1][0]), "+v"(acc[2][0]), "+v"(acc[3][0]),
                                 "+v"(acc[4][0]), "+v"(acc[5][0]), "+v"(acc[6][0]), "+v"(acc[7][0]));
        STAMP(2);
#pragma unroll
        for (int j = 0; j < 8; ++j) y[j] += acc[j][0];
        STAMP(3);
        {
            const bool k5 = g4 == 3, k3 = k5 && rt == 15;
            f32x2 s0 = {0.f, 0.f}, s1 = {0.f, 0.f};
#pragma unroll
            for (int j = 0; j < 8; ++j) {
                const float y3 = (j >= 5 ? k5 : j >= 3 ? k3 : false) ? 0.f : y[j][3];
                s0 += f32x2{y[j][0], y[j][1]}; s1 += f32x2{y[j][2], y3};
            }
            s0 += s1;
            const float m = quad_sum(s0[0] + s0[1]) * (rt == 15 ? 1.f / (L_ - 15 * TS) : 1.f / TS);
            const f32x2 mm = {m, m};
            f32x2 q0 = {0.f, 0.f}, q1 = {0.f, 0.f};
#pragma unroll
            for (int j = 0; j < 8; ++j) {
                const float y3 = (j >= 5 ? k5 : j >= 3 ? k3 : false) ? m : y[j][3];
                const f32x2 d0 = f32x2{y[j][0], y[j][1]} - mm, d1 = f32x2{y[j][2], y3} - mm;
                q0 += d0 * d0; q1 += d1 * d1;
            }
            q0 += q1;
            const float m2 = quad_sum(q0[0] + q0[1]);
            unsigned m2u = __float_as_uint(m2);
            if (m2u == 0xFFFFFFFFu) m2u = 0x7FFFFFFFu;
            if (lane < 16) st_dev(box + rt * 64 + 16 * wave + lane, ((u64)m2u << 32) | __float_as_uint(m));
            const unsigned tp = (unsigned)__builtin_amdgcn_s_memrealtime() | 1u;
            if (lane == 0) *(volatile LAS unsigned*)(L + L_TP + wave * 4) = tp;
        }
        STAMP(4);
        {
            const int word = (8 * wave + 4 * (c16 >> 3) + (c16 & 3)) ^ ((g4 & 1) << 3);
            LAS unsigned char* yp = L + YB + ((c16 >> 2) & 1) * YPL + 32 * g4 * 128 + word * 4;
#pragma unroll
            for (int j = 0; j < 8; ++j)
#pragma unroll
                for (int i = 0; i < 4; ++i) *(LAS float*)(yp + (8 * i + j) * 128) = y[j][i];
        }
        LGKM_BAR();
#pragma unroll
        for (int u = 0; u < 2; ++u)
#pragma unroll
            for (int pl = 0; pl < 2; ++pl) v[u][pl] = *(const LAS f32x4*)(yr + pl * YPL + u * 1024);
    }
    LGKM_BAR();
    STAMP(6);
    f32x4 sa[2], sc[2];
#pragma unroll
    for (int p = 0; p < 2; ++p) {
        sa[p] = *(const LAS f32x4*)(L + L_AC + c8 * 32 + p * 16);
        sc[p] = *(const LAS f32x4*)(L + L_AC + 256 + c8 * 32 + p * 16);
    }
    constexpr float K1 = -2.0f * 1.4426950408889634f * 0.7978845608028654f, K2 = K1 * 0.044715f;
#pragma unroll
    for (int u = 0; u < 2; ++u) {
        const int t = 16 * wave + 8 * u + r8;
        unsigned o[4];
#pragma unroll
        for (int h = 0; h < 4; ++h) {
            const int p = h >> 1, e = (h & 1) * 2;
            const f32x2 z = f32x2{v[u][p][e], v[u][p][e + 1]} * f32x2{sa[p][e], sa[p][e + 1]} + f32x2{sc[p][e], sc[p][e + 1]};
            const f32x2 arg = z * (z * z * K2 + K1);
            const f32x2 dn = f32x2{__builtin_amdgcn_exp2f(arg[0]), __builtin_amdgcn_exp2f(arg[1])} + 1.f;
            const f32x2 r = z * f32x2{__builtin_amdgcn_rcpf(dn[0]), __builtin_amdgcn_rcpf(dn[1])};
            o[h] = __builtin_bit_cast(unsigned, bf16x2{(__bf16)r[0], (__bf16)r[1]});
        }
        if (t < nv) {
            const u32x4 ov = u32x4{o[0], o[1], o[2], o[3]};
            u16* op = out + ((size_t)(b * L_ + l0 + t) * D_ + ct * 64 + c8 * 8);
            asm volatile("global_store_dwordx4 %0, %1, off " STPOL :: "v"(op), "v"(ov) : "memory");
        }
    }
    if (wave == 4 && bid == 101) {
        const unsigned tend = (unsigned)__builtin_amdgcn_s_memrealtime();
        const unsigned d = min(tA - tpre, 2000u) * 20;
        while ((unsigned)__builtin_amdgcn_s_memrealtime() - tend < d) __builtin_amdgcn_s_sleep(2);
    }
}

extern "C" void kernel_launch(void* const* d_in, const int* in_sizes, int n_in, void* d_out, int out_size,
                              void* d_ws, size_t ws_size, hipStream_t stream) {
    conv_stem_fused<<<256, 512, 0, stream>>>((const u16*)d_in[0], (const u16*)d_in[1], (const u16*)d_in[2],
                                             (const u16*)d_in[3], (const u16*)d_in[4], (u16*)d_out, (unsigned char*)d_ws);
}
```

```cpp
#include <hip/hip_runtime.h>
#include <stdint.h>

typedef unsigned short u16;
typedef unsigned long long u64;
typedef __attribute__((ext_vector_type(8))) __bf16 bf16x8;
typedef __attribute__((ext_vector_type(2))) __bf16 bf16x2;
typedef __attribute__((ext_vector_type(4))) short s16x4;
typedef __attribute__((ext_vector_type(8))) short s16x8;
typedef __attribute__((ext_vector_type(4))) float f32x4;
typedef __attribute__((ext_vector_type(2))) float f32x2;
typedef __attribute__((ext_vector_type(4))) unsigned u32x4;
#define LAS __attribute__((address_space(3)))
#define DI __device__ __forceinline__

constexpr int T_ = 4000, D_ = 512, L_ = 1998, TS = 125;

constexpr unsigned XS = 0;
constexpr unsigned WE = 65536;
constexpr unsigned WO = WE + 49152;
constexpr unsigned MISC = WO + 32768;
constexpr unsigned L_EP = MISC;
constexpr unsigned L_AC = MISC + 64;
constexpr unsigned L_TP = L_AC + 512;
constexpr unsigned LDS_BYTES = L_TP + 64;
constexpr unsigned EX = 0;
constexpr unsigned YB = 32768;
constexpr unsigned YPL = 16384 + 64;

constexpr size_t WS_EPOCH = 0;
constexpr size_t WS_FLAG = 4096;
constexpr size_t WS_HINT = 8192;
constexpr size_t WS_BOX = 65536;

#define ROT_EXPR (((0x02311320u >> (4 * xcd)) + 4 * ((idx >> 3) & 1)) & 7)
#define LFIRST(c) __builtin_expect(!!(c), 1)
constexpr u64 BOX_EMPTY = 0xFFFFFFFFFFFFFFFFull;
constexpr unsigned SPIN_MAX = 1u << 16;
constexpr int SPLIT = 7;
constexpr unsigned MARGIN = 22;

DI float bf2f(u16 v) { return __uint_as_float(((unsigned)v) << 16); }
DI u64 ld_dev(const u64* p) { return __hip_atomic_load(p, __ATOMIC_RELAXED, __HIP_MEMORY_SCOPE_AGENT); }
DI void st_dev(u64* p, u64 v) { __hip_atomic_store(p, v, __ATOMIC_RELAXED, __HIP_MEMORY_SCOPE_AGENT); }
DI float half_sum(float v) {
    const unsigned u = __float_as_uint(v);
    const auto r = __builtin_amdgcn_permlane32_swap(u, u, false, false);
    return __uint_as_float(r[0]) + __uint_as_float(r[1]);
}
#define BAR() asm volatile("s_barrier" ::: "memory")
#define LGKM_BAR() asm volatile("s_waitcnt lgkmcnt(0)\n\ts_barrier" ::: "memory")
#define VM_BARN(n) asm volatile("s_waitcnt vmcnt(%0)\n\ts_barrier" :: "n"(n) : "memory")
#define VM_BAR(n) asm volatile("s_waitcnt vmcnt(" #n ")\n\ts_barrier" ::: "memory")

DI void dmax(unsigned ldsaddr, unsigned voff, __amdgpu_buffer_rsrc_t srd, unsigned soff) {
    asm volatile("s_mov_b32 m0, %0\n\ts_nop 0\n\tbuffer_load_dwordx4 %1, %2, %3 offen lds"
                 :: "s"(ldsaddr), "v"(voff), "s"(srd), "s"(soff) : "m0", "memory");
}
DI void dmaw(unsigned ldsaddr, unsigned voff, __amdgpu_buffer_rsrc_t srd, unsigned soff) {
    asm volatile("s_mov_b32 m0, %0\n\ts_nop 0\n\tbuffer_load_dwordx4 %1, %2, %3 offen lds"
                 :: "s"(ldsaddr), "v"(voff), "s"(srd), "s"(soff) : "m0", "memory");
}

DI float quad_sum(float v) {
    const unsigned u = __float_as_uint(v);
    const auto r = __builtin_amdgcn_permlane16_swap(u, u, false, false);
    return half_sum(__uint_as_float(r[0]) + __uint_as_float(r[1]));
}
DI bf16x8 ld_a(const LAS unsigned char* p) { return *(const LAS bf16x8*)p; }
DI bf16x8 ld_b(const LAS unsigned char* p) {
    const s16x4 lo = __builtin_amdgcn_ds_read_tr16_b64_v4i16((LAS s16x4*)p);
    const s16x4 hi = __builtin_amdgcn_ds_read_tr16_b64_v4i16((LAS s16x4*)(p + 512));
    const s16x8 r = __builtin_shufflevector(lo, hi, 0, 1, 2, 3, 4, 5, 6, 7);
    return __builtin_bit_cast(bf16x8, r);
}

struct Op { int a, s, u; };
constexpr Op op_of(int nt, int m) {
    int k = 0;
    for (int a = 0; a < 7 + nt; ++a)
        for (int s = 0; s < nt; ++s)
            if (a - s >= 0 && a - s < 8)
                for (int u = 1; u >= 0; --u) { if (k == m) return Op{a, s, u}; ++k; }
    return Op{-1, -1, -1};
}
constexpr int BAR_E = 36, BAR_O = 18, BAR_L = 27;
constexpr int rd_even(int m) {
    switch (m) {
        case 2: return 4; case 7: return 5; case 12: return 6; case 17: return 7; case 21: return 8; case 25: return 9;
        case 36: return 21; case 37: return 20; case 38: return 10; case 39: return 11; case 40: return 23; case 41: return 22;
        case 42: return 12; case 43: return 13; case 45: return 14;
        default: return -1;
    }
}
constexpr int rd_odd(int m) {
    switch (m) {
        case 0: return 5; case 2: return 6; case 4: return 7; case 6: return 8;
        case 18: return 21; case 19: return 20; case 20: return 10; case 21: return 11; case 22: return 23; case 23: return 22;
        case 24: return 12; case 25: return 25; case 26: return 24; case 28: return 13;
        default: return -1;
    }
}
struct Ptr { unsigned xa, xb, we[2], wo[2]; };
DI const LAS unsigned char* lds_at(unsigned a) { return (const LAS unsigned char*)(size_t)a; }
template <bool ODD> DI bf16x8 rd_x(const Ptr& p, int a) {
    return a < 8 ? ld_a(lds_at(p.xa) + (ODD ? 16384 : 0) + a * 2048) : ld_a(lds_at(p.xb) + (ODD ? 16384 : 0) + (a - 8) * 2048);
}
template <bool ODD> DI bf16x8 rd_w(const Ptr& p, int s, int u) { return ld_b(lds_at(ODD ? p.wo[u] : p.we[u]) + s * 8192); }
DI void mma(f32x4& c, const bf16x8& a, const bf16x8& b) {
    asm volatile("v_mfma_f32_16x16x32_bf16 %0, %1, %2, %0" : "+v"(c) : "v"(a), "v"(b));
}
DI void flip(unsigned& a, int d) { asm volatile("v_add_u32 %0, %1, %0" : "+v"(a) : "s"(d)); }
DI void negate(int& d) { asm volatile("s_sub_i32 %0, 0, %0" : "+s"(d) :: "scc"); }
template <bool ODD, int M>
DI void stage_ops(f32x4 (&acc)[8][2], bf16x8 (&xa)[10], const bf16x8 (&wc)[3][2], bf16x8 (&wn)[3][2], Ptr& p, int& dx, int& de, int& dq) {
    constexpr int NT = ODD ? 2 : 3, NM = 16 * NT;
    if constexpr (M < NM) {
        if constexpr (M == (ODD ? BAR_O : BAR_E)) { LGKM_BAR(); __builtin_amdgcn_sched_barrier(0); }
        constexpr Op o = op_of(NT, M);
        mma(acc[o.a - o.s][o.u], xa[o.a], wc[o.s][o.u]);
        __builtin_amdgcn_sched_barrier(0);
        constexpr int rd = ODD ? rd_odd(M) : rd_even(M);
        if constexpr (rd >= 0 && rd < 10) xa[rd] = rd_x<ODD>(p, rd);
        if constexpr (rd >= 10 && rd < 20) xa[rd - 10] = rd_x<!ODD>(p, rd - 10);
        if constexpr (rd >= 20) wn[(rd - 20) >> 1][rd & 1] = rd_w<!ODD>(p, (rd - 20) >> 1, rd & 1);
        if constexpr (ODD && M == 8) flip(p.xa, dx);
        if constexpr (ODD && M == 9) flip(p.xb, dx);
        if constexpr (ODD && M == 10) flip(p.we[0], de);
        if constexpr (ODD && M == 11) flip(p.we[1], de);
        if constexpr (ODD && M == 12) flip(p.wo[0], dq);
        if constexpr (ODD && M == 13) flip(p.wo[1], dq);
        if constexpr (ODD && M == 14) { negate(dx); negate(de); }
        if constexpr (ODD && M == 15) negate(dq);
        __builtin_amdgcn_sched_barrier(0);
        stage_ops<ODD, M + 1>(acc, xa, wc, wn, p, dx, de, dq);
    }
}
template <int M>
DI void stage_last(f32x4 (&acc)[8][2], bf16x8 (&xa)[10], const bf16x8 (&wc)[3][2], f32x4 (&rc)[8], const Ptr& p,
                   LAS unsigned char* snd, const LAS unsigned char* rcv) {
    if constexpr (M < 32) {
        if constexpr (M == BAR_L) { LGKM_BAR(); __builtin_amdgcn_sched_barrier(0); }
        constexpr Op o = op_of(2, 2 * (M & 15));
        constexpr int u = M < 16 ? 1 : 0;
        mma(acc[o.a - o.s][u], xa[o.a], wc[o.s][u]);
        __builtin_amdgcn_sched_barrier(0);
        if constexpr (M < 4) xa[5 + M] = rd_x<true>(p, 5 + M);
        if constexpr (M >= 10 && M <= 22 && (M & 1) == 0) *(LAS f32x4*)(snd + ((M - 10) >> 1) * 1024) = acc[(M - 10) >> 1][1];
        if constexpr (M == 23) *(LAS f32x4*)(snd + 7 * 1024) = acc[7][1];
        if constexpr (M >= BAR_L) {
            constexpr int n0 = (M - BAR_L) * 8 / (32 - BAR_L), n1 = (M + 1 - BAR_L) * 8 / (32 - BAR_L);
#pragma unroll
            for (int j = n0; j < n1; ++j) rc[j] = *(const LAS f32x4*)(rcv + j * 1024);
        }
        __builtin_amdgcn_sched_barrier(0);
        stage_last<M + 1>(acc, xa, wc, rc, p, snd, rcv);
    }
}

__global__ void __launch_bounds__(512, 2)
conv_stem_fused(const u16* __restrict__ x, const u16* __restrict__ w, const u16* __restrict__ bias,
                const u16* __restrict__ gamma, const u16* __restrict__ beta, u16* __restrict__ out,
                unsigned char* __restrict__ ws) {
    __shared__ __attribute__((aligned(16))) unsigned char smem[LDS_BYTES];
    LAS unsigned char* L = (LAS unsigned char*)smem;
    const unsigned lbase = (unsigned)(size_t)L;

    const int tid = threadIdx.x, lane = tid & 63;
    const int wave = __builtin_amdgcn_readfirstlane(tid >> 6);
    int bid = blockIdx.x;
    const int xcd = bid & 7, idx = bid >> 3;
    const int R = 8 * (xcd >> 1) + (idx & 7), ct = 4 * (xcd & 1) + ((idx ^ (idx >> 3)) & 3);
    const int b = R >> 4, rt = R & 15;
    const int l0 = rt * TS, nv = rt == 15 ? L_ - 15 * TS : TS;

    const int r8 = tid >> 3 & 7, c8 = tid & 7;
    const int lw = wave & 3;
    const unsigned rot = ROT_EXPR;
    unsigned vx[4];
#pragma unroll
    for (int n = 0; n < 4; ++n) {
        const int p = min(l0 + 64 * (lw & 1) + 8 * r8 + 2 * n + (lw >> 1), T_ / 2 - 1);
        vx[n] = p * 2048 + ((c8 ^ (r8 & 6)) << 4);
    }
    unsigned vw = r8 * 1024 + ((c8 ^ (r8 & 2) ^ ((lw & 1) << 2)) << 4);
    const unsigned sx0 = b * (T_ * D_ * 2), sw0 = ct * 128 + lw * 8192;
    unsigned mx0 = lbase + XS + lw * 1024;
    unsigned so_first = sx0 + rot * 128, wo_first = sw0 + rot * 65536;
    int c16 = lane & 15, g4 = lane >> 4;
    unsigned yoff = lbase + YB + (16 * wave + r8) * 128 + ((c8 ^ (wave & 2)) << 4);
    int is_loader = wave >= 4;
    unsigned ooff = (16 * wave + r8) * 1024 + c8 * 16, aoff = lbase + L_AC + c8 * 32;
    asm volatile("" : "+v"(ooff), "+v"(aoff));
    asm volatile("" : "+v"(vx[0]), "+v"(vx[1]), "+v"(vx[2]), "+v"(vx[3]), "+v"(vw), "+v"(c16), "+v"(g4), "+v"(yoff));
    asm volatile("" : "+s"(mx0), "+s"(so_first), "+s"(wo_first));
    asm volatile("" :: "s"(x), "s"(w), "s"(bias), "s"(gamma), "s"(beta), "s"(out), "s"(ws));
    const __amdgpu_buffer_rsrc_t sx = __builtin_amdgcn_make_buffer_rsrc((void*)x, 0, 2 * T_ * D_ * 2, 0x00020000);
    const __amdgpu_buffer_rsrc_t sw = __builtin_amdgcn_make_buffer_rsrc((void*)w, 0, 5 * D_ * D_ * 2, 0x00020000);
    f32x4 v[2][2];
    const LAS unsigned char* const yr = (const LAS unsigned char*)(size_t)yoff;
    u64 *box, *flags, *epoch, *hint;
    auto ws_ptrs = [&]() {
        int grp = b * 8 + ct;
        asm volatile("" : "+s"(grp));
        box = (u64*)(ws + WS_BOX) + grp * 1024;
        flags = (u64*)(ws + WS_FLAG) + grp * 16;
        epoch = (u64*)(ws + WS_EPOCH + grp * 256);
        hint = (u64*)(ws + WS_HINT) + grp * 256;
    };
    if (LFIRST(is_loader)) {
        const unsigned mwe0 = mx0 + (WE - XS), mwo0 = mx0 + (WO - XS);
        auto issue_e = [&](int cc, int lo, int hi) {
            const int cr = (cc + rot) & 7;
            const unsigned so = cc == 0 ? so_first : sx0 + cr * 128, mx = mx0 + (cc & 1) * 32768, mw = mwe0 + (cc & 1) * 24576;
            const unsigned wo = cc == 0 ? wo_first : sw0 + cr * 65536;
#pragma unroll
            for (int n = 0; n < 4; ++n) if (n >= lo && n < hi) dmax(mx + n * 4096, vx[n], sx, so);
#pragma unroll
            for (int ti = 0; ti < 3; ++ti)
#pragma unroll
                for (int h = 0; h < 2; ++h) if (4 + 2 * ti + h >= lo && 4 + 2 * ti + h < hi) dmaw(mw + ti * 8192 + h * 4096, vw, sw, wo + ti * 2 * 524288 + h * 32768);
        };
        auto issue_o = [&](int cc) {
            const int cr = (cc + rot) & 7;
            const unsigned so = sx0 + cr * 128 + 1024, mx = mx0 + (cc & 1) * 32768 + 16384, mw = mwo0 + (cc & 1) * 16384, wo = sw0 + cr * 65536 + 524288;
#pragma unroll
            for (int n = 0; n < 4; ++n) dmax(mx + n * 4096, vx[n], sx, so);
#pragma unroll
            for (int ti = 0; ti < 2; ++ti)
#pragma unroll
                for (int h = 0; h < 2; ++h) dmaw(mw + ti * 8192 + h * 4096, vw, sw, wo + ti * 2 * 524288 + h * 32768);
        };
        issue_e(0, 0, 10); issue_o(0);
        ws_ptrs();
        VM_BAR(8);
        issue_e(1, 0, 10); VM_BAR(10);
        issue_o(1); VM_BAR(8);
        issue_e(2, 0, 10); issue_o(2); VM_BAR(18);
        issue_e(3, 0, SPLIT); VM_BARN(8 + SPLIT);
        for (int cc = 2; cc < 6; ++cc) {
            issue_e(cc + 1, SPLIT, 10); issue_o(cc + 1); VM_BAR(18);
            issue_e(cc + 2, 0, SPLIT); VM_BARN(8 + SPLIT);
        }
        issue_e(7, SPLIT, 10); issue_o(7); VM_BAR(18);
        unsigned gam16, bet16;
        {
            const u16* gp = gamma + ct * 64 + 16 * lw + c16;
            const u16* bp = beta + ct * 64 + 16 * lw + c16;
            asm volatile("global_load_ushort %0, %2, off\n\tglobal_load_ushort %1, %3, off" : "=&v"(gam16), "=&v"(bet16) : "v"(gp), "v"(bp) : "memory");
        }
        VM_BAR(10);
        VM_BAR(2);
        asm volatile("s_waitcnt vmcnt(0)" : "+v"(gam16), "+v"(bet16) :: "memory");
        const u64 e1 = *(volatile LAS u64*)(L + L_EP) + 1;
        u64 fl = lane < 16 ? ld_dev(flags + lane) : e1;
        const unsigned th = (unsigned)__builtin_amdgcn_s_memrealtime();
        if (lw == 0 && lane == 0) st_dev(hint + rt * 16, (e1 << 32) | th);
        BAR();
        for (unsigned sp = 0; __any(fl != e1) && sp < SPIN_MAX; ++sp) fl = lane < 16 ? ld_dev(flags + lane) : e1;
        if (rt == 0 && lw == 0 && lane == 0) st_dev(epoch, e1);
        volatile LAS unsigned* tpw = (volatile LAS unsigned*)(L + L_TP);
        if (lw == 0) {
            u64 h = (e1 << 32) | th;
            for (unsigned sp = 0; sp < SPIN_MAX; ++sp) {
                if (lane < 16) h = ld_dev(hint + lane * 16);
                if (!__any((unsigned)(h >> 32) != (unsigned)e1)) break;
            }
            int d = (unsigned)(h >> 32) == (unsigned)e1 ? (int)((unsigned)h - th) : 0;
#pragma unroll
            for (int o = 1; o < 16; o <<= 1) d = max(d, __shfl_xor(d, o));
            d = min(max(d, 0), 1000);
            if (lane == 0) tpw[4] = 0x80000000u | (unsigned)d;
        }
        const u64* src = box + g4 * 256 + 16 * lw + c16;
        const u64* srcr = box + 16 * lw + c16;
        asm volatile("" : "+v"(src), "+v"(srcr));
        float gam = bf2f((u16)gam16), bet = bf2f((u16)bet16);
        unsigned acw = lbase + L_AC + (16 * lw + c16) * 4;
        float a3 = (g4 == 3 ? (float)(TS - (16 * TS - L_)) : (float)TS) * (1.f / L_);
        asm volatile("" : "+v"(gam), "+v"(bet), "+v"(acw), "+v"(a3));
        unsigned tp = 0, dl = 0;
#pragma unroll 1
        for (unsigned sp = 0; sp < SPIN_MAX * 16; ++sp) {
            tp = tpw[lw]; dl = tpw[4];
            if (tp != 0 && dl != 0) break;
            __builtin_amdgcn_s_sleep(1);
        }
        const unsigned tstart = tp + (dl & 0xFFFFu) + MARGIN;
        while ((int)((unsigned)__builtin_amdgcn_s_memrealtime() - tstart) < 0) {}
        u64 g[4];
        u64 gr;
        asm volatile("global_load_dwordx2 %0, %5, off sc1\n\tglobal_load_dwordx2 %1, %5, off offset:512 sc1\n\t"
                     "global_load_dwordx2 %2, %5, off offset:1024 sc1\n\tglobal_load_dwordx2 %3, %5, off offset:1536 sc1\n\t"
                     "global_load_dwordx2 %4, %6, off sc1"
                     : "=&v"(g[0]), "=&v"(g[1]), "=&v"(g[2]), "=&v"(g[3]), "=&v"(gr) : "v"(src), "v"(srcr) : "memory");
        BAR();
#pragma unroll
        for (int u = 0; u < 2; ++u)
#pragma unroll
            for (int p = 0; p < 2; ++p) v[u][p] = *(const LAS f32x4*)(yr + p * YPL + u * 1024);
        {
            unsigned cnt = SPIN_MAX;
            u64 msk;
            const u64 lim = 0xFFFFFFFEFFFFFFFFull;
            asm volatile(
                "1:\n\t"
                "s_waitcnt vmcnt(4)\n\tv_cmp_lt_u64 %[m], %[lim], %[g0]\n\t"
                "s_waitcnt vmcnt(3)\n\tv_cmp_lt_u64 vcc, %[lim], %[g1]\n\ts_or_b64 %[m], %[m], vcc\n\t"
                "s_waitcnt vmcnt(2)\n\tv_cmp_lt_u64 vcc, %[lim], %[g2]\n\ts_or_b64 %[m], %[m], vcc\n\t"
                "s_waitcnt vmcnt(1)\n\tv_cmp_lt_u64 vcc, %[lim], %[g3]\n\ts_or_b64 %[m], %[m], vcc\n\t"
                "s_waitcnt vmcnt(0)\n\tv_cmp_lt_u64 vcc, %[lim], %[gr]\n\ts_or_b64 %[m], %[m], vcc\n\t"
                "s_cbranch_scc0 2f\n\t"
                "s_sub_u32 %[cnt], %[cnt], 1\n\t"
                "s_cbranch_scc1 2f\n\t"
                "global_load_dwordx2 %[g0], %[p], off sc1\n\t"
                "global_load_dwordx2 %[g1], %[p], off offset:512 sc1\n\t"
                "global_load_dwordx2 %[g2], %[p], off offset:1024 sc1\n\t"
                "global_load_dwordx2 %[g3], %[p], off offset:1536 sc1\n\t"
                "global_load_dwordx2 %[gr], %[pr], off sc1\n\t"
                "s_branch 1b\n"
                "2:"
                : [g0] "+v"(g[0]), [g1] "+v"(g[1]), [g2] "+v"(g[2]), [g3] "+v"(g[3]), [gr] "+v"(gr), [m] "=&s"(msk), [cnt] "+s"(cnt)
                : [lim] "s"(lim), [p] "v"(src), [pr] "v"(srcr) : "vcc", "scc", "memory");
        }
        const float ref = __uint_as_float((unsigned)gr);
        const float d0 = __uint_as_float((unsigned)g[0]) - ref, d1 = __uint_as_float((unsigned)g[1]) - ref;
        const float d2 = __uint_as_float((unsigned)g[2]) - ref, d3 = __uint_as_float((unsigned)g[3]) - ref;
        const float h = (__uint_as_float((unsigned)(g[0] >> 32)) + __uint_as_float((unsigned)(g[1] >> 32)))
                      + (__uint_as_float((unsigned)(g[2] >> 32)) + __uint_as_float((unsigned)(g[3] >> 32)));
        constexpr float A_ = (float)TS / L_;
        const float e3 = a3 * d3;
        const float dm = quad_sum(fmaf(A_, d0 + d1 + d2, e3));
        const float qq = quad_sum(fmaf(A_, fmaf(d2, d2, fmaf(d1, d1, d0 * d0)), fmaf(e3, d3, fmaf(h, 1.f / L_, 0.25e-5f))));
        const float mean = ref + dm;
        const float sc = gam * __builtin_amdgcn_rsqf(fmaf(-dm, dm, qq));
        *(LAS float*)(size_t)acw = sc;
        *(LAS float*)(size_t)(acw + 256) = bet - mean * sc;
    } else {
        const int kh = wave & 1, chh = wave >> 1;
        ws_ptrs();
        u64 e0 = 0;
        if (wave == 0) e0 = ld_dev(epoch);
        unsigned b16 = bias[ct * 64 + 16 * wave + c16];
        asm volatile("" : "+v"(b16), "+v"(e0));
        if (wave == 0) {
            st_dev(box + rt * 64 + lane, BOX_EMPTY);
            if (lane < 5) *(volatile LAS unsigned*)(L + L_TP + lane * 4) = 0;
        }
        const float b0 = bf2f((u16)b16);
        f32x4 acc[8][2];
#pragma unroll
        for (int j = 0; j < 8; ++j) {
            acc[j][0] = f32x4{b0, b0, b0, b0}; acc[j][1] = f32x4{0.f, 0.f, 0.f, 0.f};
            asm volatile("" : "+v"(acc[j][0]), "+v"(acc[j][1]));
        }
        Ptr p;
        p.xa = lbase + XS + c16 * 128 + (((4 * kh + g4) ^ (c16 & 6)) << 4);
        p.xb = lbase + XS + (c16 + 1) * 128 + (((4 * kh + g4) ^ ((c16 + 1) & 6)) << 4);
        {
            const int q4 = c16 >> 2, p4 = lane & 3;
#pragma unroll
            for (int u = 0; u < 2; ++u) {
                const unsigned wl = kh * 4096 + (8 * g4 + q4) * 128 + (((4 * chh + 2 * (u ^ kh) + (p4 >> 1)) ^ (q4 & 2) ^ ((g4 & 1) << 2)) << 4) + 8 * (p4 & 1);
                p.we[u] = lbase + WE + wl; p.wo[u] = lbase + WO + wl;
            }
        }
        asm volatile("" : "+v"(p.xa), "+v"(p.xb), "+v"(p.we[0]), "+v"(p.we[1]), "+v"(p.wo[0]), "+v"(p.wo[1]));
        LAS unsigned char* const snd = L + EX + wave * 8192 + lane * 16;
        bf16x8 xa[10], we[3][2], wo[3][2];
        BAR();
#define SB() __builtin_amdgcn_sched_barrier(0)
        we[0][1] = rd_w<false>(p, 0, 1); SB(); we[0][0] = rd_w<false>(p, 0, 0); SB();
        xa[0] = rd_x<false>(p, 0); SB(); xa[1] = rd_x<false>(p, 1); SB();
        we[1][1] = rd_w<false>(p, 1, 1); SB(); we[1][0] = rd_w<false>(p, 1, 0); SB();
        xa[2] = rd_x<false>(p, 2); SB();
        we[2][1] = rd_w<false>(p, 2, 1); SB(); we[2][0] = rd_w<false>(p, 2, 0); SB();
        xa[3] = rd_x<false>(p, 3); SB();
#undef SB
        f32x4 y[8];
        const LAS unsigned char* const rcv = L + EX + (wave ^ 1) * 8192 + lane * 16;
        int dx = 32768, de = 24576, dq = 16384;
#pragma unroll 1
        for (int it = 0; it < 7; ++it) {
            stage_ops<false, 0>(acc, xa, we, wo, p, dx, de, dq);
            stage_ops<true, 0>(acc, xa, wo, we, p, dx, de, dq);
            int itv = it;
            asm volatile("" : "+s"(itv));
            if (itv == 1) {
                asm volatile("s_waitcnt vmcnt(0)" : "+v"(e0) :: "memory");
                if (wave == 0 && lane == 0) { *(volatile LAS u64*)(L + L_EP) = e0; st_dev(flags + rt, e0 + 1); }
            }
        }
        f32x2 k5m = {1.f, g4 == 3 ? 0.f : 1.f}, k3m = {1.f, (g4 == 3 && rt == 15) ? 0.f : 1.f};
        float inv_nv = rt == 15 ? 1.f / (L_ - 15 * TS) : 1.f / TS;
        unsigned puboff = c16 * 8;
        const u64* pub = box + rt * 64 + 16 * wave;
        asm volatile("" : "+v"(k5m), "+v"(k3m), "+v"(puboff), "+s"(pub), "+v"(inv_nv));
        stage_ops<false, 0>(acc, xa, we, wo, p, dx, de, dq);
        stage_last<0>(acc, xa, wo, y, p, snd, rcv);
        asm volatile("s_nop 7" : "+v"(acc[0][0]), "+v"(acc[1][0]), "+v"(acc[2][0]), "+v"(acc[3][0]),
                                 "+v"(acc[4][0]), "+v"(acc[5][0]), "+v"(acc[6][0]), "+v"(acc[7][0]));
#pragma unroll
        for (int j = 0; j < 8; ++j) y[j] += acc[j][0];
        {
            f32x2 s0 = f32x2{y[0][0], y[0][1]}, s1 = f32x2{y[0][2], y[0][3]};
#pragma unroll
            for (int j = 1; j < 8; ++j) {
                s0 += f32x2{y[j][0], y[j][1]};
                if (j < 3) s1 += f32x2{y[j][2], y[j][3]};
                else s1 = __builtin_elementwise_fma(f32x2{y[j][2], y[j][3]}, j < 5 ? k3m : k5m, s1);
            }
            s0 += s1;
            const float m = quad_sum(s0[0] + s0[1]) * inv_nv;
            const f32x2 mm = {m, m}, m3 = mm * k3m, m5 = mm * k5m;
            f32x2 q0, q1;
#pragma unroll
            for (int j = 0; j < 8; ++j) {
                const f32x2 y23 = {y[j][2], y[j][3]};
                const f32x2 d0 = f32x2{y[j][0], y[j][1]} - mm;
                const f32x2 d1 = j < 3 ? y23 - mm : j < 5 ? __builtin_elementwise_fma(y23, k3m, -m3) : __builtin_elementwise_fma(y23, k5m, -m5);
                if (j == 0) { q0 = d0 * d0; q1 = d1 * d1; } else { q0 += d0 * d0; q1 += d1 * d1; }
            }
            q0 += q1;
            const float m2 = quad_sum(q0[0] + q0[1]);
            const unsigned m2u = min(__float_as_uint(m2), 0xFFFFFFFEu);
            asm volatile("global_store_dwordx2 %0, %1, %2 sc1" :: "v"(puboff), "v"(((u64)m2u << 32) | __float_as_uint(m)), "s"(pub) : "memory");
            const unsigned tp = (unsigned)__builtin_amdgcn_s_memrealtime() | 1u;
            if (lane == 0) *(volatile LAS unsigned*)(L + L_TP + wave * 4) = tp;
        }
        {
            const int word = (8 * wave + 4 * (c16 >> 3) + (c16 & 3)) ^ ((g4 & 1) << 3);
            LAS unsigned char* yp = L + YB + ((c16 >> 2) & 1) * YPL + 32 * g4 * 128 + word * 4;
#pragma unroll
            for (int j = 0; j < 8; ++j)
#pragma unroll
                for (int i = 0; i < 4; ++i) *(LAS float*)(yp + (8 * i + j) * 128) = y[j][i];
        }
        LGKM_BAR();
#pragma unroll
        for (int u = 0; u < 2; ++u)
#pragma unroll
            for (int pl = 0; pl < 2; ++pl) v[u][pl] = *(const LAS f32x4*)(yr + pl * YPL + u * 1024);
    }
    LGKM_BAR();
    f32x4 sa[2], sc[2];
#pragma unroll
    for (int p = 0; p < 2; ++p) {
        sa[p] = *(const LAS f32x4*)(lds_at(aoff) + p * 16);
        sc[p] = *(const LAS f32x4*)(lds_at(aoff) + 256 + p * 16);
    }
    const u16* const ob = out + ((size_t)(b * L_ + l0) * D_ + ct * 64);
    constexpr float K1 = -2.0f * 1.4426950408889634f * 0.7978845608028654f, K2 = K1 * 0.044715f;
#pragma unroll
    for (int u = 0; u < 2; ++u) {
        const int t = 16 * wave + 8 * u + r8;
        unsigned o[4];
#pragma unroll
        for (int h = 0; h < 4; ++h) {
            const int p = h >> 1, e = (h & 1) * 2;
            const f32x2 z = f32x2{v[u][p][e], v[u][p][e + 1]} * f32x2{sa[p][e], sa[p][e + 1]} + f32x2{sc[p][e], sc[p][e + 1]};
            const f32x2 arg = z * (z * z * K2 + K1);
            const f32x2 dn = f32x2{__builtin_amdgcn_exp2f(arg[0]), __builtin_amdgcn_exp2f(arg[1])} + 1.f;
            const f32x2 r = z * f32x2{__builtin_amdgcn_rcpf(dn[0]), __builtin_amdgcn_rcpf(dn[1])};
            o[h] = __builtin_bit_cast(unsigned, bf16x2{(__bf16)r[0], (__bf16)r[1]});
        }
        if (t < nv) {
            const u32x4 ov = u32x4{o[0], o[1], o[2], o[3]};
            asm volatile("global_store_dwordx4 %0, %1, %2 sc1" :: "v"(ooff + u * 8192), "v"(ov), "s"(ob) : "memory");
        }
    }
}

extern "C" void kernel_launch(void* const* d_in, const int* in_sizes, int n_in, void* d_out, int out_size,
                              void* d_ws, size_t ws_size, hipStream_t stream) {
    conv_stem_fused<<<256, 512, 0, stream>>>((const u16*)d_in[0], (const u16*)d_in[1], (const u16*)d_in[2],
                                             (const u16*)d_in[3], (const u16*)d_in[4], (u16*)d_out, (unsigned char*)d_ws);
}
```
